```python
import torch

def _softmax(x, axis=-1):
    return torch.softmax(x, dim=axis)

def _gelu(x):
    return torch.nn.functional.gelu(x, approximate='tanh')

def _silu(x):
    return torch.nn.functional.silu(x)

def _sigmoid(x):
    return torch.sigmoid(x)

def _layernorm(x, axis=-1, eps=1e-05):
    m = x.mean(dim=axis, keepdim=True)
    v = x.var(dim=axis, keepdim=True, unbiased=False)
    return (x - m) / torch.sqrt(v + eps)

def _scatter_add(tgt, idx, src, axis=0):
    return tgt.clone().index_add_(axis, idx, src)

def _segment_sum(src, seg, n):
    out = torch.zeros((n,) + src.shape[1:], dtype=src.dtype)
    return out.index_add_(0, seg, src)

def _pad(x, pw):
    flat = tuple((v for pair in reversed(pw) for v in pair))
    return torch.nn.functional.pad(x, flat)
kernel_name = 'gated_mix__memory_aug_recurrent_mem_tr__ssm_conv_s4__prompt'

def setup_inputs():

    _w = lambda a: (lambda t: t.to(torch.bfloat16) if t.is_floating_point() else t)(torch.as_tensor(a))

    def _A_setup():
        B, S, Dm = (1, 2048, 512)
        H, Dh = (32, 128)
        M, seg = (8, 32)
        seg = min(seg, S)
        S = S // seg * seg
        return {'x': _w((lambda *s: torch.randn(*s, dtype=torch.bfloat16) * 0.05)(B, S, Dm)), 'mem0': _w((lambda *s: torch.randn(*s, dtype=torch.bfloat16) * 0.4)(B, M, Dm)), 'Wq': _w((lambda *s: torch.randn(*s, dtype=torch.bfloat16) * 6.4)(Dm, H, Dh) * Dm ** (-0.5)), 'Wk': _w((lambda *s: torch.randn(*s, dtype=torch.bfloat16) * 6.4)(Dm, H, Dh) * Dm ** (-0.5)), 'Wv': _w((lambda *s: torch.randn(*s, dtype=torch.bfloat16) * 0.4)(Dm, H, Dh) * Dm ** (-0.5)), 'Wo': _w((lambda *s: torch.randn(*s, dtype=torch.bfloat16) * 0.4)(H, Dh, Dm) * (H * Dh) ** (-0.5)), 'seg_len': seg}
    _A_in = _A_setup()

    def _B_setup():
        B, L, D, N = (1, 2048, 512, 128)
        A_re = -(lambda _x: torch.exp(torch.as_tensor(_x)))((lambda *s: torch.randn(*s, dtype=torch.bfloat16) * 6.4)(D, N))
        A_im = torch.pi * (lambda *s: torch.randn(*s, dtype=torch.bfloat16) * 0.05)(D, N)
        B_re = (lambda *s: torch.randn(*s, dtype=torch.bfloat16) * 0.1)(D, N)
        B_im = (lambda *s: torch.randn(*s, dtype=torch.bfloat16) * 0.1)(D, N)
        C_re = (lambda *s: torch.randn(*s, dtype=torch.bfloat16) * 0.1)(D, N)
        C_im = (lambda *s: torch.randn(*s, dtype=torch.bfloat16) * 0.1)(D, N)
        log_dt = (lambda *s: torch.randn(*s, dtype=torch.bfloat16) * 0.05)(D) * 0.5 - 4.0
        Dskip = (lambda *s: torch.randn(*s, dtype=torch.bfloat16) * 0.05)(D)
        return {'u': _w((lambda *s: torch.randn(*s, dtype=torch.bfloat16) * 0.05)(B, L, D)), 'A_re': _w(A_re), 'A_im': _w(A_im), 'B_re': _w(B_re), 'B_im': _w(B_im), 'C_re': _w(C_re), 'C_im': _w(C_im), 'log_dt': _w(log_dt), 'Dskip': _w(Dskip)}
    _B_in = _B_setup()
    return {'A_x': _A_in['x'], 'A_mem0': _A_in['mem0'], 'A_Wq': _A_in['Wq'], 'A_Wk': _A_in['Wk'], 'A_Wv': _A_in['Wv'], 'A_Wo': _A_in['Wo'], 'A_seg_len': _A_in['seg_len'], 'B_u': _B_in['u'], 'B_A_re': _B_in['A_re'], 'B_A_im': _B_in['A_im'], 'B_B_re': _B_in['B_re'], 'B_B_im': _B_in['B_im'], 'B_C_re': _B_in['C_re'], 'B_C_im': _B_in['C_im'], 'B_log_dt': _B_in['log_dt'], 'B_Dskip': _B_in['Dskip'], 'gate_w': _w((lambda *s: torch.randn(*s, dtype=torch.bfloat16) * 0.05)(512) * 16.0)}

def reference(A_x, A_mem0, A_Wq, A_Wk, A_Wv, A_Wo, A_seg_len, B_u, B_A_re, B_A_im, B_B_re, B_B_im, B_C_re, B_C_im, B_log_dt, B_Dskip, gate_w):

    def _A_ref(x, mem0, Wq, Wk, Wv, Wo, seg_len):
        B, S, Dm = x.shape
        H, Dh = (Wq.shape[1], Wq.shape[2])
        M = mem0.shape[1]
        scale = Dh ** (-0.5)
        n_seg = S // seg_len
        L = M + seg_len
        idx = torch.arange(L)
        mask = (idx[:, None] < M) | (idx[:, None] >= idx[None, :])
        mem = mem0
        outs = []
        for s in range(n_seg):
            xs = x[:, s * seg_len:(s + 1) * seg_len, :].clone()
            h = torch.concatenate([mem, xs], axis=1)
            q = torch.einsum('btd,dhe->bhte', h, Wq)
            k = torch.einsum('btd,dhe->bhte', h, Wk)
            v = torch.einsum('btd,dhe->bhte', h, Wv)
            scores = torch.einsum('bhqe,bhke->bhqk', q, k) * scale
            scores = torch.where(mask[None, None], scores, -1000000000.0)
            p = _softmax(scores, axis=-1)
            o = torch.einsum('bhqk,bhke->bhqe', p, v)
            o = torch.einsum('bhte,hed->btd', o, Wo)
            mem = o[:, :M, :].clone()
            outs.append(o[:, M:, :])
        return torch.concatenate(outs, axis=1)

    def _B_ref(u, A_re, A_im, B_re, B_im, C_re, C_im, log_dt, Dskip):
        Bsz, L, D = u.shape
        dt = torch.exp(log_dt)[:, None]
        A = A_re + 1j * A_im
        Bc = B_re + 1j * B_im
        Cc = C_re + 1j * C_im
        logdA = dt * A
        dB = dt * Bc
        l = (lambda x, dt: x.to(dt))(torch.arange(L), A_re.dtype)
        P = torch.exp(logdA[:, :, None] * l[None, None, :])
        K = torch.einsum('dn,dnl,dn->dl', Cc, P, dB).real
        L2 = 2 * L
        Kp = _pad(K, ((0, 0), (0, L)))
        up = (lambda x, *d: x.permute(*d))(u, 0, 2, 1)
        up = _pad(up, ((0, 0), (0, 0), (0, L)))
        Kf = (lambda x, axes=(-2, -1): torch.fft.rfft2(x.float(), dim=axes))(Kp[:, None, :], axes=(-2, -1))
        Uf = (lambda x, axes=(-2, -1): torch.fft.rfft2(x.float(), dim=axes))(up[:, :, None, :], axes=(-2, -1))
        Yf = Uf * Kf[None, :, :, :]
        y = (lambda x, s, axes=(-2, -1): torch.fft.irfft2(x, s=s, dim=axes).to(torch.bfloat16))(Yf, (1, L2), axes=(-2, -1))[:, :, 0, :L]
        y = (lambda x, *d: x.permute(*d))(y, 0, 2, 1) + u * Dskip[None, None, :]
        return y
    x = A_x
    g = _sigmoid((x * gate_w).sum(-1, keepdims=True))
    a = _A_ref(x, A_mem0, A_Wq, A_Wk, A_Wv, A_Wo, A_seg_len)
    b = _B_ref(x, B_A_re, B_A_im, B_B_re, B_B_im, B_C_re, B_C_im, B_log_dt, B_Dskip)
    return g * a + (1 - g) * b
```

```cpp
#include <hip/hip_runtime.h>
#include <hip/hip_bf16.h>
#include <stdint.h>

typedef unsigned v4u __attribute__((ext_vector_type(4)));
typedef unsigned v2u __attribute__((ext_vector_type(2)));
typedef float f4 __attribute__((ext_vector_type(4)));
typedef float f2 __attribute__((ext_vector_type(2)));
typedef __bf16 bf8 __attribute__((ext_vector_type(8)));
typedef __bf16 bf16x16 __attribute__((ext_vector_type(16)));
typedef __bf16 bf2 __attribute__((ext_vector_type(2)));
typedef short s4 __attribute__((ext_vector_type(4)));
typedef unsigned v8u __attribute__((ext_vector_type(8)));
#define LAS __attribute__((address_space(3)))
typedef __amdgpu_buffer_rsrc_t rsrc_t;

constexpr unsigned SENT = 0xFFFFFFFFu;
constexpr int AUX_SC1 = 16;
constexpr unsigned OFF_CTR = 0;
constexpr unsigned CTR_INIT0 = 0, CTR_INIT1 = 256, CTR_SSM = 512;
constexpr unsigned OFF_UXF = 4096;
constexpr unsigned OFF_PXF = 16384;
constexpr unsigned CTR_BYTES = 32768;
constexpr unsigned OFF_STEP = 65536;
constexpr unsigned ST_MEM = 0;
constexpr unsigned ST_P = 65536;
constexpr unsigned ST_UM = 65536 + 163840;
constexpr unsigned STEP_BYTES = 65536 + 163840 + 262144;
constexpr unsigned OFF_UX = OFF_STEP + 64u * STEP_BYTES;
constexpr unsigned OFF_PX = OFF_UX + 64u * 1048576u;
constexpr unsigned OFF_YB = OFF_PX + 64u * 81920u;
constexpr unsigned OFF_G = OFF_YB + 2097152u;
constexpr unsigned WS_BYTES = OFF_G + 8192u;
constexpr int INIT0_STEPS = 4;
constexpr int NSPARE = 160;
constexpr unsigned SPIN_CAP = 400000u;
constexpr int LDS_BYTES = 136 * 1024;

struct Params {
  const unsigned short* x; const unsigned short* mem0; const unsigned short* Wq; const unsigned short* Wk;
  const unsigned short* Wv; const unsigned short* Wo; const unsigned short* Are; const unsigned short* Aim;
  const unsigned short* Bre; const unsigned short* Bim; const unsigned short* Cre; const unsigned short* Cim;
  const unsigned short* logdt; const unsigned short* Dsk; const unsigned short* gw;
  unsigned short* out; unsigned char* ws;
};

__device__ __forceinline__ float bf2f(unsigned short u) { return __uint_as_float((unsigned)u << 16); }
__device__ __forceinline__ unsigned pk2(float lo, float hi) {
  bf2 v; v[0] = (__bf16)lo; v[1] = (__bf16)hi; return __builtin_bit_cast(unsigned, v);
}
__device__ __forceinline__ float rbf(float f) { return (float)(__bf16)f; }
__device__ __forceinline__ unsigned short f2bfu(float f) { return __builtin_bit_cast(unsigned short, (__bf16)f); }
__device__ __forceinline__ unsigned san(unsigned u) { return u < 0xFFFFFFFEu ? u : 0xFFFFFFFEu; }
__device__ __forceinline__ v4u ld16(rsrc_t rs, unsigned off) { return __builtin_amdgcn_raw_buffer_load_b128(rs, off, 0, AUX_SC1); }
__device__ __forceinline__ unsigned ld4(rsrc_t rs, unsigned off) { return __builtin_amdgcn_raw_buffer_load_b32(rs, off, 0, AUX_SC1); }
__device__ __forceinline__ v2u ld8(rsrc_t rs, unsigned off) { return __builtin_amdgcn_raw_buffer_load_b64(rs, off, 0, AUX_SC1); }
__device__ __forceinline__ void st16(rsrc_t rs, unsigned off, v4u v) { __builtin_amdgcn_raw_buffer_store_b128(v, rs, off, 0, AUX_SC1); }
__device__ __forceinline__ void st8(rsrc_t rs, unsigned off, v2u v) { __builtin_amdgcn_raw_buffer_store_b64(v, rs, off, 0, AUX_SC1); }
__device__ __forceinline__ void st4(rsrc_t rs, unsigned off, unsigned v) { __builtin_amdgcn_raw_buffer_store_b32(v, rs, off, 0, AUX_SC1); }
__device__ __forceinline__ void cbar() { asm volatile("" ::: "memory"); }
__device__ __forceinline__ void drain() { asm volatile("s_waitcnt vmcnt(0)" ::: "memory"); }
__device__ __forceinline__ void wgbar() {
  asm volatile("s_waitcnt lgkmcnt(0)" ::: "memory"); __builtin_amdgcn_s_barrier(); asm volatile("" ::: "memory");
}
__device__ __forceinline__ unsigned mx4(v4u v) { return max(max(v.x, v.y), max(v.z, v.w)); }
__device__ __forceinline__ v4u lds16(const LAS unsigned char* p) { return *(const LAS v4u*)p; }
__device__ __forceinline__ v2u ldstr(const LAS unsigned char* p) {
  return __builtin_bit_cast(v2u, __builtin_amdgcn_ds_read_tr16_b64_v4i16((LAS s4*)p));
}
__device__ __forceinline__ bf8 asbf8(v4u v) { return __builtin_bit_cast(bf8, v); }
__device__ __forceinline__ f4 mfma16(v4u a, v4u b, f4 c) { return __builtin_amdgcn_mfma_f32_16x16x32_bf16(asbf8(a), asbf8(b), c, 0, 0, 0); }
__device__ __forceinline__ f4 smf(v4u a, v4u b0, v4u b1, f4 c, int idx) {
  v8u b = {b0.x, b0.y, b0.z, b0.w, b1.x, b1.y, b1.z, b1.w};
  return __builtin_amdgcn_smfmac_f32_16x16x64_bf16(asbf8(a), __builtin_bit_cast(bf16x16, b), c, idx, 0, 0);
}
__device__ __forceinline__ float x32(float v) {
  unsigned u = __float_as_uint(v);
  auto r = __builtin_amdgcn_permlane32_swap(u, u, false, false);
  return __uint_as_float((threadIdx.x & 32) ? r[0] : r[1]);
}
__device__ __forceinline__ float add32(float v) { unsigned u = __float_as_uint(v); auto r = __builtin_amdgcn_permlane32_swap(u, u, false, false); return __uint_as_float(r[0]) + __uint_as_float(r[1]); }
__device__ __forceinline__ float max32(float v) { unsigned u = __float_as_uint(v); auto r = __builtin_amdgcn_permlane32_swap(u, u, false, false); return fmaxf(__uint_as_float(r[0]), __uint_as_float(r[1])); }
__device__ __forceinline__ float add16(float v) { unsigned u = __float_as_uint(v); auto r = __builtin_amdgcn_permlane16_swap(u, u, false, false); return __uint_as_float(r[0]) + __uint_as_float(r[1]); }
__device__ __forceinline__ float max16(float v) { unsigned u = __float_as_uint(v); auto r = __builtin_amdgcn_permlane16_swap(u, u, false, false); return fmaxf(__uint_as_float(r[0]), __uint_as_float(r[1])); }
__device__ __forceinline__ float addror8(float v) {
  int r = __builtin_amdgcn_update_dpp(0, __float_as_int(v), 0x128, 0xF, 0xF, false);
  return v + __int_as_float(r);
}
__device__ __forceinline__ float fold2(float a, float b) {
  auto r = __builtin_amdgcn_permlane32_swap(__float_as_uint(a), __float_as_uint(b), false, false);
  return __uint_as_float(r[0]) + __uint_as_float(r[1]);
}

__device__ __forceinline__ void wait_ctr(rsrc_t rs, unsigned off, unsigned target, bool& dead) {
  unsigned spins = dead ? SPIN_CAP : 0;
  while (true) {
    cbar();
    unsigned v = ld4(rs, off);
    if (__builtin_amdgcn_readfirstlane(v) >= target) { cbar(); return; }
    if (++spins > SPIN_CAP) { dead = true; return; }
    __builtin_amdgcn_s_sleep(8);
  }
}
__device__ __forceinline__ void wait_flags(rsrc_t rs, unsigned off, bool& dead) {
  unsigned spins = dead ? SPIN_CAP : 0;
  while (true) {
    cbar();
    unsigned v = ld4(rs, off);
    if (__all(v != 0u)) { cbar(); return; }
    if (++spins > SPIN_CAP) { dead = true; return; }
    __builtin_amdgcn_s_sleep(4);
  }
}
template <int C>
__device__ __forceinline__ void poll16(rsrc_t rs, const unsigned (&off)[C], v4u (&v)[C], bool& dead) {
  unsigned spins = dead ? SPIN_CAP : 0;
  while (true) {
    cbar();
#pragma unroll
    for (int c = 0; c < C; c++) v[c] = ld16(rs, off[c]);
    unsigned m = 0;
#pragma unroll
    for (int c = 0; c < C; c++) m = max(m, mx4(v[c]));
    if (__all(m != SENT)) return;
    if (++spins > SPIN_CAP) { dead = true; return; }
  }
}

template <int NCT>
__device__ __forceinline__ void wload_issue(const unsigned short* src, int rowstride, int lane, v4u (&t)[NCT * 2]) {
  constexpr int LPR = NCT * 2;
  constexpr int RPI = 64 / LPR;
#pragma unroll
  for (int i = 0; i < NCT * 2; i++) {
    int row = i * RPI + lane / LPR, pc = lane % LPR;
    t[i] = *(const v4u*)(src + (size_t)row * rowstride + pc * 8);
  }
}
template <int NCT>
__device__ __forceinline__ void wload_finish(LAS unsigned char* slab, int lane, const v4u (&t)[NCT * 2], v4u (&f)[NCT][2]) {
  constexpr int LPR = NCT * 2, RPI = 64 / LPR, RB = NCT * 32;
#pragma unroll
  for (int i = 0; i < NCT * 2; i++) {
    int row = i * RPI + lane / LPR, pc = lane % LPR;
    *(LAS v4u*)(slab + row * RB + pc * 16) = t[i];
  }
  asm volatile("s_waitcnt lgkmcnt(0)" ::: "memory");
  int gb = lane >> 4, q = (lane >> 2) & 3, p = lane & 3;
#pragma unroll
  for (int ct = 0; ct < NCT; ct++)
#pragma unroll
    for (int hf = 0; hf < 2; hf++) {
      v2u a = ldstr(slab + (32 * hf + 8 * gb + q) * RB + ct * 32 + p * 8);
      v2u b = ldstr(slab + (32 * hf + 8 * gb + 4 + q) * RB + ct * 32 + p * 8);
      f[ct][hf] = v4u{a.x, a.y, b.x, b.y};
    }
  asm volatile("s_waitcnt lgkmcnt(0)" ::: "memory");
}

constexpr int XS_STRIDE = 1040;
constexpr int L_XS = 0;
constexpr int L_MEM = 33280;
constexpr int L_QKT = 41472;
constexpr int L_QXT = 49664;
constexpr int L_KXT = 57856;
constexpr int L_KXP = 66048;
constexpr int L_SLAB = 0;

__device__ __forceinline__ void x_issue(const Params& p, int s, int tid, v4u (&t)[4]) {
#pragma unroll
  for (int i = 0; i < 4; i++) t[i] = *(const v4u*)(p.x + (size_t)s * 16384 + (i * 512 + tid) * 8);
}
__device__ __forceinline__ void x_store(LAS unsigned char* lds, int tid, const v4u (&t)[4]) {
#pragma unroll
  for (int i = 0; i < 4; i++) { int c = i * 512 + tid; *(LAS v4u*)(lds + L_XS + (c >> 6) * XS_STRIDE + (c & 63) * 16) = t[i]; }
}
__device__ __forceinline__ v4u mem0_chunk(const Params& p, int w, int lane) {
  int r = lane & 15, ga = lane >> 4;
  const unsigned* m = (const unsigned*)(p.mem0 + (r & 7) * 512 + 64 * w + 16 * ga + 2 * (r >> 3));
  return v4u{m[0], m[2], m[4], m[6]};
}

constexpr float SCL2 = 0.08838834764831845f * 1.4426950408889634f;

__device__ __forceinline__ void role_qk(const Params& p, LAS unsigned char* lds, rsrc_t rs, int h) {
  const int tid = threadIdx.x, lane = tid & 63, w = __builtin_amdgcn_readfirstlane(tid >> 6);
  const int c16 = lane & 15, g = lane >> 4, tq = (lane >> 2) & 3, tp = lane & 3;
  const int idx = (c16 < 8) ? 0x4444 : 0xEEEE;
  const unsigned cp = (unsigned)(blockIdx.x & 7);
  bool dead = false;
  v4u Wf[8][2][2];
  {
    const unsigned short* src = (w < 4 ? p.Wq : p.Wk) + h * 128 + 32 * (w & 3);
    v4u t[8][4];
#pragma unroll
    for (int k = 0; k < 8; k++) wload_issue<2>(src + (size_t)k * 64 * 4096, 4096, lane, t[k]);
#pragma unroll
    for (int k = 0; k < 8; k++) wload_finish<2>(lds + L_SLAB + w * 8192, lane, t[k], Wf[k]);
  }
  v4u xt[4];
  x_issue(p, 0, tid, xt);
  wgbar();
  x_store(lds, tid, xt);
  if (w == 7) wait_ctr(rs, OFF_CTR + CTR_INIT0, NSPARE, dead);
  wgbar();

  v4u fr[8];
  f4 sxx[2];
  for (int s = 0; s < 64; s++) {
    {
      f4 acc[2][2];
#pragma unroll
      for (int rt = 0; rt < 2; rt++)
#pragma unroll
        for (int ct = 0; ct < 2; ct++) acc[rt][ct] = f4{0.f, 0.f, 0.f, 0.f};
#pragma unroll
      for (int ks = 0; ks < 16; ks++) {
#pragma unroll
        for (int rt = 0; rt < 2; rt++) {
          v4u a = lds16(lds + L_XS + (16 * rt + c16) * XS_STRIDE + ks * 64 + g * 16);
#pragma unroll
          for (int ct = 0; ct < 2; ct++) acc[rt][ct] = mfma16(a, Wf[ks >> 1][ct][ks & 1], acc[rt][ct]);
        }
      }
#pragma unroll
      for (int rt = 0; rt < 2; rt++)
#pragma unroll
        for (int ct = 0; ct < 2; ct++) {
          int e = 32 * (w & 3) + 16 * ct + c16;
          unsigned p0 = pk2(acc[rt][ct][0], acc[rt][ct][1]), p1 = pk2(acc[rt][ct][2], acc[rt][ct][3]);
          *(LAS v2u*)(lds + (w < 4 ? L_QXT : L_KXT) + e * 64 + (16 * rt + 4 * g) * 2) = v2u{p0, p1};
          if (w >= 4) {
            int pos = (g & 1) * 16 + (g >> 1) * 8 + 2 * rt;
            *(LAS unsigned*)(lds + L_KXP + e * 64 + pos * 2) = p0;
            *(LAS unsigned*)(lds + L_KXP + e * 64 + (pos + 4) * 2) = p1;
          }
        }
    }
    if (s < 63) x_issue(p, s + 1, tid, xt);
    wgbar();
    if (w < 4) {
#pragma unroll
      for (int tau = 0; tau < 2; tau++)
#pragma unroll
        for (int ks = 0; ks < 4; ks++) {
          v2u a = ldstr(lds + L_KXP + (32 * ks + 8 * g + tq) * 64 + tau * 32 + tp * 8);
          v2u b = ldstr(lds + L_KXP + (32 * ks + 8 * g + 4 + tq) * 64 + tau * 32 + tp * 8);
          fr[tau * 4 + ks] = v4u{a.x, a.y, b.x, b.y};
        }
    } else if (w < 6) {
      const int rt = w - 4;
      sxx[0] = f4{0.f, 0.f, 0.f, 0.f}; sxx[1] = sxx[0];
#pragma unroll
      for (int ks = 0; ks < 4; ks++) {
        v2u a = ldstr(lds + L_QXT + (32 * ks + 8 * g + tq) * 64 + rt * 32 + tp * 8);
        v2u b = ldstr(lds + L_QXT + (32 * ks + 8 * g + 4 + tq) * 64 + rt * 32 + tp * 8);
        fr[ks] = v4u{a.x, a.y, b.x, b.y};
#pragma unroll
        for (int tau = 0; tau < 2; tau++) {
          v2u c = ldstr(lds + L_KXT + (32 * ks + 8 * g + tq) * 64 + tau * 32 + tp * 8);
          v2u d = ldstr(lds + L_KXT + (32 * ks + 8 * g + 4 + tq) * 64 + tau * 32 + tp * 8);
          sxx[tau] = mfma16(v4u{c.x, c.y, d.x, d.y}, fr[ks], sxx[tau]);
        }
      }
    } else if (w == 7 && s == INIT0_STEPS - 1) {
      wait_ctr(rs, OFF_CTR + CTR_INIT1, NSPARE, dead);
    }
    if (s < 63) x_store(lds, tid, xt);
    const unsigned sb = OFF_STEP + (unsigned)s * STEP_BYTES;
    {
      v4u m[1];
      if (s == 0) m[0] = mem0_chunk(p, w, lane);
      else { const unsigned o[1] = {sb + ST_MEM + cp * 8192u + (unsigned)tid * 16u}; poll16<1>(rs, o, m, dead); }
      *(LAS v4u*)(lds + L_MEM + tid * 16) = m[0];
    }
    wgbar();
    {
      f4 acc[2] = {f4{0.f, 0.f, 0.f, 0.f}, f4{0.f, 0.f, 0.f, 0.f}};
#pragma unroll
      for (int t = 0; t < 8; t++) {
        v4u a = lds16(lds + L_MEM + t * 1024 + lane * 16);
#pragma unroll
        for (int ct = 0; ct < 2; ct++) acc[ct] = smf(a, Wf[t][ct][0], Wf[t][ct][1], acc[ct], idx);
      }
#pragma unroll
      for (int ct = 0; ct < 2; ct++)
        *(LAS v2u*)(lds + L_QKT + (32 * w + 16 * ct + c16) * 32 + g * 8) =
            v2u{pk2(acc[ct][0], acc[ct][1]), pk2(acc[ct][2], acc[ct][3])};
    }
    wgbar();
    if (w < 4) {
      if (s < 63) {
        f4 sx0 = {0.f, 0.f, 0.f, 0.f}, sx1 = sx0, sm = sx0;
#pragma unroll
        for (int ks = 0; ks < 4; ks++) {
          v2u a = ldstr(lds + L_QKT + (32 * ks + 8 * g + tq) * 32 + tp * 8);
          v2u b = ldstr(lds + L_QKT + (32 * ks + 8 * g + 4 + tq) * 32 + tp * 8);
          v2u c = ldstr(lds + L_QKT + (128 + 32 * ks + 8 * g + tq) * 32 + tp * 8);
          v2u d = ldstr(lds + L_QKT + (128 + 32 * ks + 8 * g + 4 + tq) * 32 + tp * 8);
          v4u qf = {a.x, a.y, b.x, b.y};
          sx0 = mfma16(fr[ks], qf, sx0);
          sx1 = mfma16(fr[4 + ks], qf, sx1);
          sm = mfma16(v4u{c.x, c.y, d.x, d.y}, qf, sm);
        }
        float v[12];
#pragma unroll
        for (int i = 0; i < 4; i++) { v[i] = addror8(sx0[i]); v[4 + i] = addror8(sx1[i]); v[8 + i] = add32(addror8(sm[i])); }
        float mx = v[0];
#pragma unroll
        for (int i = 1; i < 12; i++) mx = fmaxf(mx, v[i]);
        mx = max32(max16(mx));
        float sum = 0.f, summ = 0.f;
#pragma unroll
        for (int i = 0; i < 12; i++) { v[i] = __builtin_amdgcn_exp2f((v[i] - mx) * SCL2); if (i < 8) sum += v[i]; else summ += v[i]; }
        sum += 0.5f * summ;
        sum = add32(add16(sum));
        float inv = __builtin_amdgcn_rcpf(sum);
        v4u px = {san(pk2(v[0] * inv, v[1] * inv)), san(pk2(v[2] * inv, v[3] * inv)), san(pk2(v[4] * inv, v[5] * inv)), san(pk2(v[6] * inv, v[7] * inv))};
        v2u pm = {san(pk2(v[8] * inv, v[9] * inv)), san(pk2(v[10] * inv, v[11] * inv))};
        const unsigned pb = sb + ST_P + (unsigned)(2 * w + (c16 >> 3)) * 20480u;
        st16(rs, pb + (unsigned)(h * 512 + (g * 8 + (c16 & 7)) * 16), px);
        if (g < 2) st8(rs, pb + 16384u + (unsigned)((((h >> 1) * 16) + g * 8 + (c16 & 7)) * 16 + (h & 1) * 8), pm);
      }
    } else if (w < 6) {
      const int rt = w - 4;
      f4 sm = {0.f, 0.f, 0.f, 0.f};
#pragma unroll
      for (int ks = 0; ks < 4; ks++) {
        v2u c = ldstr(lds + L_QKT + (128 + 32 * ks + 8 * g + tq) * 32 + tp * 8);
        v2u d = ldstr(lds + L_QKT + (128 + 32 * ks + 8 * g + 4 + tq) * 32 + tp * 8);
        sm = mfma16(v4u{c.x, c.y, d.x, d.y}, fr[ks], sm);
      }
      float v[12];
      const int R = 16 * rt + c16;
#pragma unroll
      for (int i = 0; i < 4; i++) {
        v[i] = (4 * g + i <= R) ? sxx[0][i] : -3.0e38f;
        v[4 + i] = (16 + 4 * g + i <= R) ? sxx[1][i] : -3.0e38f;
        v[8 + i] = add32(sm[i]);
      }
      float mx = v[0];
#pragma unroll
      for (int i = 1; i < 12; i++) mx = fmaxf(mx, v[i]);
      mx = max32(max16(mx));
      float sum = 0.f, summ = 0.f;
#pragma unroll
      for (int i = 0; i < 12; i++) {
        float e = __builtin_amdgcn_exp2f((v[i] - mx) * SCL2);
        if (i < 4) e = (4 * g + i <= R) ? e : 0.f; else if (i < 8) e = (12 + 4 * g + i <= R) ? e : 0.f;
        v[i] = e; if (i < 8) sum += e; else summ += e;
      }
      sum += 0.5f * summ;
      sum = add32(add16(sum));
      float inv = __builtin_amdgcn_rcpf(sum);
      v4u px = {pk2(v[0] * inv, v[1] * inv), pk2(v[2] * inv, v[3] * inv), pk2(v[4] * inv, v[5] * inv), pk2(v[6] * inv, v[7] * inv)};
      unsigned pm = (g < 2) ? pk2(v[8] * inv, v[9] * inv) : pk2(v[10] * inv, v[11] * inv);
      const unsigned xb = OFF_PX + (unsigned)s * 81920u;
      st16(rs, xb + (unsigned)((h * 2 + rt) * 1024 + lane * 16), px);
      st4(rs, xb + 65536u + (unsigned)((((h >> 2) * 2 + rt) * 64 + (h & 3) * 16 + c16) * 16 + g * 4), pm);
      drain();
      if (lane == 0) st4(rs, OFF_PXF + (unsigned)(s * 64 + h * 2 + rt) * 4u, 1u);
    }
  }
}

__device__ __forceinline__ void role_vo(const Params& p, LAS unsigned char* lds, rsrc_t rs, int h) {
  const int tid = threadIdx.x, lane = tid & 63, w = __builtin_amdgcn_readfirstlane(tid >> 6);
  const int c16 = lane & 15, g = lane >> 4, tq = (lane >> 2) & 3, tp = lane & 3;
  const int idx = (c16 < 8) ? 0x4444 : 0xEEEE;
  const unsigned cp = (unsigned)(blockIdx.x & 7);
  bool dead = false;
  v4u Vf[8][1][2], Of[2][4][2];
  {
    const unsigned short* sv = p.Wv + h * 128 + 16 * w;
    const unsigned short* so = p.Wo + (size_t)h * 65536 + 64 * w;
    v4u t[8][2], t2[2][8];
#pragma unroll
    for (int k = 0; k < 8; k++) wload_issue<1>(sv + (size_t)k * 64 * 4096, 4096, lane, t[k]);
#pragma unroll
    for (int k = 0; k < 2; k++) wload_issue<4>(so + (size_t)k * 64 * 512, 512, lane, t2[k]);
#pragma unroll
    for (int k = 0; k < 8; k++) wload_finish<1>(lds + L_SLAB + w * 8192, lane, t[k], Vf[k]);
#pragma unroll
    for (int k = 0; k < 2; k++) wload_finish<4>(lds + L_SLAB + w * 8192, lane, t2[k], Of[k]);
  }
  v4u xt[4];
  x_issue(p, 0, tid, xt);
  wgbar();
  x_store(lds, tid, xt);
  if (w == 7) wait_ctr(rs, OFF_CTR + CTR_INIT0, NSPARE, dead);
  wgbar();
  for (int s = 0; s < 64; s++) {
    {
      f4 acc[2] = {f4{0.f, 0.f, 0.f, 0.f}, f4{0.f, 0.f, 0.f, 0.f}};
#pragma unroll
      for (int ks = 0; ks < 16; ks++)
#pragma unroll
        for (int rt = 0; rt < 2; rt++) {
          v4u a = lds16(lds + L_XS + (16 * rt + c16) * XS_STRIDE + ks * 64 + g * 16);
          acc[rt] = mfma16(a, Vf[ks >> 1][0][ks & 1], acc[rt]);
        }
#pragma unroll
      for (int rt = 0; rt < 2; rt++)
        *(LAS v2u*)(lds + L_QXT + (16 * w + c16) * 64 + (16 * rt + 4 * g) * 2) = v2u{pk2(acc[rt][0], acc[rt][1]), pk2(acc[rt][2], acc[rt][3])};
    }
    if (s < 63) x_issue(p, s + 1, tid, xt);
    wgbar();
    {
      f4 acc[2][4];
#pragma unroll
      for (int rt = 0; rt < 2; rt++)
#pragma unroll
        for (int ct = 0; ct < 4; ct++) acc[rt][ct] = f4{0.f, 0.f, 0.f, 0.f};
#pragma unroll
      for (int ks = 0; ks < 4; ks++)
#pragma unroll
        for (int rt = 0; rt < 2; rt++) {
          v2u a = ldstr(lds + L_QXT + (32 * ks + 8 * g + tq) * 64 + rt * 32 + tp * 8);
          v2u b = ldstr(lds + L_QXT + (32 * ks + 8 * g + 4 + tq) * 64 + rt * 32 + tp * 8);
          v4u af = {a.x, a.y, b.x, b.y};
#pragma unroll
          for (int ct = 0; ct < 4; ct++) acc[rt][ct] = mfma16(af, Of[ks >> 1][ct][ks & 1], acc[rt][ct]);
        }
      if (s < 63) x_store(lds, tid, xt);
#pragma unroll
      for (int ct = 0; ct < 4; ct++) {
        v4u u = {pk2(acc[0][ct][0], acc[0][ct][1]), pk2(acc[0][ct][2], acc[0][ct][3]), pk2(acc[1][ct][0], acc[1][ct][1]), pk2(acc[1][ct][2], acc[1][ct][3])};
        st16(rs, OFF_UX + (unsigned)s * 1048576u + (unsigned)(((4 * w + ct) * 32 + h) * 1024 + lane * 16), u);
      }
      drain();
    }
    if (w == 7 && s == INIT0_STEPS - 1) wait_ctr(rs, OFF_CTR + CTR_INIT1, NSPARE, dead);
    wgbar();
    if (tid == 0) st4(rs, OFF_UXF + (unsigned)(s * 32 + h) * 4u, 1u);
    const unsigned sb = OFF_STEP + (unsigned)s * STEP_BYTES;
    {
      v4u m[1];
      if (s == 0) m[0] = mem0_chunk(p, w, lane);
      else { const unsigned o[1] = {sb + ST_MEM + cp * 8192u + (unsigned)tid * 16u}; poll16<1>(rs, o, m, dead); }
      *(LAS v4u*)(lds + L_MEM + tid * 16) = m[0];
    }
    wgbar();
    {
      f4 a0 = {0.f, 0.f, 0.f, 0.f}, a1 = a0;
#pragma unroll
      for (int t = 0; t < 8; t += 2) {
        a0 = smf(lds16(lds + L_MEM + t * 1024 + lane * 16), Vf[t][0][0], Vf[t][0][1], a0, idx);
        a1 = smf(lds16(lds + L_MEM + (t + 1) * 1024 + lane * 16), Vf[t + 1][0][0], Vf[t + 1][0][1], a1, idx);
      }
      a0 += a1;
      float f0 = fold2(a0[0], a0[1]), f1 = fold2(a0[2], a0[3]);
      const int gp = g & 1, up = g >> 1;
      const int hf = (c16 >> 1) & 1, j = 2 * (c16 >> 2) + (c16 & 1);
      LAS unsigned char* b = lds + L_QKT + (w * 16 + hf * 8 + 2 * gp + up) * 16 + j * 2;
      *(LAS unsigned short*)(b) = f2bfu(f0);
      *(LAS unsigned short*)(b + 64) = f2bfu(f1);
    }
    wgbar();
    {
      v4u a0 = lds16(lds + L_QKT + lane * 16), a1 = lds16(lds + L_QKT + 1024 + lane * 16);
      f4 acc[4];
#pragma unroll
      for (int ct = 0; ct < 4; ct++) {
        acc[ct] = smf(a0, Of[0][ct][0], Of[0][ct][1], f4{0.f, 0.f, 0.f, 0.f}, idx);
        acc[ct] = smf(a1, Of[1][ct][0], Of[1][ct][1], acc[ct], idx);
      }
#pragma unroll
      for (int ct = 0; ct < 4; ct += 2) {
        float f[4];
#pragma unroll
        for (int i = 0; i < 4; i++) f[i] = fold2(acc[ct][i], acc[ct + 1][i]);
        v2u u = {san(pk2(f[0], f[1])), san(pk2(f[2], f[3]))};
        st8(rs, sb + ST_UM + (unsigned)((((4 * w + ct + (g >> 1)) * 32 + h) * 16 + c16) * 16 + (g & 1) * 8), u);
      }
    }
  }
}

__device__ __forceinline__ void role_r(const Params& p, LAS unsigned char* lds, rsrc_t rs, int c) {
  const int tid = threadIdx.x, lane = tid & 63, w = __builtin_amdgcn_readfirstlane(tid >> 6);
  const int c16 = lane & 15, g = lane >> 4;
  const int idx = (c16 < 8) ? 0x4444 : 0xEEEE;
  const unsigned cp = (unsigned)(blockIdx.x & 7);
  bool dead = false;
  if (w == 7) wait_ctr(rs, OFF_CTR + CTR_INIT0, NSPARE, dead);
  wgbar();
  for (int s = 0; s < 63; s++) {
    const unsigned sb = OFF_STEP + (unsigned)s * STEP_BYTES;
    if (s == INIT0_STEPS - 2) wait_ctr(rs, OFF_CTR + CTR_INIT1, NSPARE, dead);
    wait_flags(rs, OFF_UXF + (unsigned)(s * 32 + 4 * w + (lane & 3)) * 4u, dead);
    v4u ux[4];
#pragma unroll
    for (int i = 0; i < 4; i++) ux[i] = ld16(rs, OFF_UX + (unsigned)s * 1048576u + (unsigned)((c * 32 + 4 * w + i) * 1024 + lane * 16));
    f4 acc = {0.f, 0.f, 0.f, 0.f};
    const unsigned pb = sb + ST_P + cp * 20480u;
    if (w < 4) {
      v4u um[2];
      { const unsigned o[2] = {sb + ST_UM + (unsigned)(((c * 32 + 8 * w + g) * 16 + c16) * 16), sb + ST_UM + (unsigned)(((c * 32 + 8 * w + 4 + g) * 16 + c16) * 16)};
        poll16<2>(rs, o, um, dead); }
      v4u pa[3];
      { const unsigned o[3] = {pb + (unsigned)(2 * w * 1024 + lane * 16), pb + (unsigned)((2 * w + 1) * 1024 + lane * 16), pb + 16384u + (unsigned)(w * 1024 + lane * 16)};
        poll16<3>(rs, o, pa, dead); }
      acc = smf(pa[0], ux[0], ux[1], acc, idx);
      acc = smf(pa[1], ux[2], ux[3], acc, idx);
      acc = smf(pa[2], um[0], um[1], acc, idx);
    } else {
      v4u pa[2];
      { const unsigned o[2] = {pb + (unsigned)(2 * w * 1024 + lane * 16), pb + (unsigned)((2 * w + 1) * 1024 + lane * 16)};
        poll16<2>(rs, o, pa, dead); }
      acc = smf(pa[0], ux[0], ux[1], acc, idx);
      acc = smf(pa[1], ux[2], ux[3], acc, idx);
    }
    {
      float f0 = fold2(acc[0], acc[1]), f1 = fold2(acc[2], acc[3]);
      LAS float* red = (LAS float*)(lds + (s & 1) * 4096) + w * 128 + (4 * (g & 1) + (g >> 1)) * 16 + c16;
      red[0] = f0; red[32] = f1;
    }
    wgbar();
    {
      const int r = lane >> 2, dq = lane & 3;
      const LAS f2* red = (const LAS f2*)((LAS float*)(lds + (s & 1) * 4096) + (r & 7) * 16 + 4 * dq + 2 * (r >> 3));
      f2 a = red[0], b = red[64], c2 = red[128], d = red[192], e = red[256], f = red[320], gg = red[384], hh = red[448];
      f2 t = ((a + b) + (c2 + d)) + ((e + f) + (gg + hh));
      st4(rs, sb + STEP_BYTES + ST_MEM + (unsigned)(w * 8192 + c * 256 + lane * 4), san(pk2(t[0], t[1])));
    }
  }
}

constexpr int S_UC = 4288;
constexpr int S_RV = 4640;
constexpr int S_CH = 4 * S_UC + 2 * S_RV;
constexpr int S_KD = 4 * S_CH;
constexpr int LAGMAX = 2064;

__device__ __forceinline__ void ctr_add(const Params& p, unsigned off) {
  __hip_atomic_fetch_add((unsigned*)(p.ws + off), 1u, __ATOMIC_RELAXED, __HIP_MEMORY_SCOPE_AGENT);
}
__device__ __forceinline__ void cexp2(float a2, float brev, float L, float& re, float& im) {
  float mag = __builtin_amdgcn_exp2f(a2 * L);
  float ph = __builtin_amdgcn_fractf(brev * L);
  re = mag * __builtin_amdgcn_cosf(ph); im = mag * __builtin_amdgcn_sinf(ph);
}

__device__ __forceinline__ void ssm_unit(const Params& p, LAS unsigned char* lds, rsrc_t rs, int d0) {
  const int tid = threadIdx.x, lane = tid & 63, w = __builtin_amdgcn_readfirstlane(tid >> 6);
  const int c16 = lane & 15, g = lane >> 4, ch = w >> 1, hw = w & 1, d = d0 + ch;
  for (int i = tid; i < 4 * S_CH / 16; i += 512) *(LAS v4u*)(lds + i * 16) = v4u{0u, 0u, 0u, 0u};
  v2u xv[4];
#pragma unroll
  for (int i = 0; i < 4; i++) xv[i] = *(const v2u*)(p.x + (size_t)(i * 512 + tid) * 512 + d0);
  wgbar();
#pragma unroll
  for (int i = 0; i < 4; i++) {
    const int l = i * 512 + tid;
#pragma unroll
    for (int c = 0; c < 4; c++) {
      unsigned short v = (unsigned short)((c & 2 ? xv[i].y : xv[i].x) >> (16 * (c & 1)));
#pragma unroll
      for (int r = 0; r < 4; r++) *(LAS unsigned short*)(lds + c * S_CH + r * S_UC + (l + 32 - r) * 2) = v;
    }
  }
  {
    const float dt = rbf(expf(bf2f(p.logdt[d])));
    float L1[3];
    L1[0] = (float)((c16 & 7) + 128 * (c16 >> 3));
    L1[1] = (c16 < 8) ? (float)(256 + 128 * (c16 >> 2) + 2 * (c16 & 3)) : (float)(512 + 128 * ((c16 - 8) >> 1) + 4 * (c16 & 1));
    L1[2] = (float)(1024 + 128 * (c16 < 9 ? c16 : 0));
    const float L2 = (float)(8 * c16);
    f4 acc[3] = {f4{0.f, 0.f, 0.f, 0.f}, f4{0.f, 0.f, 0.f, 0.f}, f4{0.f, 0.f, 0.f, 0.f}};
    for (int m = 0; m < 16; m++) {
      const int n = d * 128 + 4 * (hw * 16 + m) + g;
      const float a = dt * bf2f(p.Are[n]), b = dt * bf2f(p.Aim[n]);
      const float br = dt * bf2f(p.Bre[n]), bi = dt * bf2f(p.Bim[n]), cr = bf2f(p.Cre[n]), ci = bf2f(p.Cim[n]);
      const float cbr = cr * br - ci * bi, cbi = cr * bi + ci * br;
      const float a2 = a * 1.4426950408889634f, brev = b * 0.15915494309189535f;
      float yr, yi; cexp2(a2, brev, L2, yr, yi);
#pragma unroll
      for (int t = 0; t < 3; t++) {
        float er, ei; cexp2(a2, brev, L1[t], er, ei);
        float xr = cbr * er - cbi * ei, xi = cbr * ei + cbi * er;
        acc[t] = __builtin_amdgcn_mfma_f32_16x16x4f32(xr, yr, acc[t], 0, 0, 0);
        acc[t] = __builtin_amdgcn_mfma_f32_16x16x4f32(-xi, yi, acc[t], 0, 0, 0);
      }
    }
    LAS float* kd = (LAS float*)(lds + S_KD + w * 3072);
#pragma unroll
    for (int t = 0; t < 3; t++)
#pragma unroll
      for (int i = 0; i < 4; i++) kd[(16 * t + 4 * g + i) * 16 + c16] = acc[t][i];
  }
  wgbar();
#pragma unroll
  for (int i = 0; i < 4; i++) {
    const int l = i * 512 + tid;
    const int lr = (int)rbf((float)l);
    int row, j;
    if (lr < 256) { j = (lr >> 3) & 15; row = (lr & 7) + 8 * (lr >> 7); }
    else if (lr < 512) { int x = lr - 256; j = (x & 127) >> 3; row = 16 + 4 * (x >> 7) + ((x & 7) >> 1); }
    else if (lr < 1024) { int x = lr - 512; j = (x & 127) >> 3; row = 24 + 2 * (x >> 7) + ((x & 7) >> 2); }
    else { int x = lr - 1024; j = (x & 127) >> 3; row = 32 + (x >> 7); }
#pragma unroll
    for (int c = 0; c < 4; c++) {
      const LAS float* kd = (const LAS float*)(lds + S_KD + c * 6144);
      float k = kd[row * 16 + j] + kd[768 + row * 16 + j];
      float hi = rbf(k);
      LAS unsigned char* rv = lds + c * S_CH + 4 * S_UC + (LAGMAX - l) * 2;
      *(LAS unsigned short*)(rv) = f2bfu(hi);
      *(LAS unsigned short*)(rv + S_RV) = f2bfu(k - hi);
    }
  }
  wgbar();
  f4 acc[4];
#pragma unroll
  for (int t = 0; t < 4; t++) acc[t] = f4{0.f, 0.f, 0.f, 0.f};
  const int A0 = hw, A1 = 3 - hw, A2 = 4 + hw, A3 = 7 - hw;
  const LAS unsigned char* ap = lds + ch * S_CH + 4 * S_UC + (LAGMAX - 16 * c16 + 224 + 8 * g) * 2;
  const LAS unsigned char* bp = lds + ch * S_CH + (c16 & 3) * S_UC + ((c16 & 12) + 224 + 8 * g + 32) * 2;
#define SSM_STEP(T, A) { const LAS v2u* q = (const LAS v2u*)(bp + 512 * (A) - 64 * n); v2u b0 = q[0], b1 = q[1]; v4u bf = {b0.x, b0.y, b1.x, b1.y}; \
    acc[T] = mfma16(ah, bf, acc[T]); acc[T] = mfma16(al, bf, acc[T]); }
  int n = 0;
  for (; n <= 8 * A0 + 8; n++) { v4u ah = lds16(ap - 64 * n), al = lds16(ap - 64 * n + S_RV); SSM_STEP(0, A0) SSM_STEP(1, A1) SSM_STEP(2, A2) SSM_STEP(3, A3) }
  for (; n <= 8 * A1 + 8; n++) { v4u ah = lds16(ap - 64 * n), al = lds16(ap - 64 * n + S_RV); SSM_STEP(1, A1) SSM_STEP(2, A2) SSM_STEP(3, A3) }
  for (; n <= 8 * A2 + 8; n++) { v4u ah = lds16(ap - 64 * n), al = lds16(ap - 64 * n + S_RV); SSM_STEP(2, A2) SSM_STEP(3, A3) }
  for (; n <= 8 * A3 + 8; n++) { v4u ah = lds16(ap - 64 * n), al = lds16(ap - 64 * n + S_RV); SSM_STEP(3, A3) }
#undef SSM_STEP
  {
    const float dsk = bf2f(p.Dsk[d]);
#pragma unroll
    for (int t = 0; t < 4; t++) {
      const int A = t == 0 ? A0 : t == 1 ? A1 : t == 2 ? A2 : A3;
#pragma unroll
      for (int i = 0; i < 4; i++) {
        const int l = 256 * A + 16 * (4 * g + i) + c16;
        float u = bf2f(*(const LAS unsigned short*)(lds + ch * S_CH + (l + 32) * 2));
        float b = rbf(acc[t][i]) + rbf(u * dsk);
        *(LAS unsigned short*)(lds + S_KD + l * 8 + ch * 2) = f2bfu(b);
      }
    }
  }
  wgbar();
#pragma unroll
  for (int i = 0; i < 4; i++) {
    const int l = i * 512 + tid;
    st8(rs, OFF_YB + (unsigned)(l * 1024 + d0 * 2), *(const LAS v2u*)(lds + S_KD + l * 8));
  }
}

__device__ __forceinline__ void gate_unit(const Params& p, rsrc_t rs, int r0) {
  const int tid = threadIdx.x, lane = tid & 63, w = tid >> 6;
  v4u gw = *(const v4u*)(p.gw + lane * 8);
  for (int i = 0; i < 8; i++) {
    const int row = r0 + w * 8 + i;
    v4u xv = *(const v4u*)(p.x + (size_t)row * 512 + lane * 8);
    float s = 0.f;
#pragma unroll
    for (int k = 0; k < 4; k++) {
      s += rbf(__uint_as_float(xv[k] << 16) * __uint_as_float(gw[k] << 16));
      s += rbf(__uint_as_float(xv[k] & 0xFFFF0000u) * __uint_as_float(gw[k] & 0xFFFF0000u));
    }
#pragma unroll
    for (int o = 32; o > 0; o >>= 1) s += __shfl_xor(s, o);
    s = rbf(s);
    float gg = rbf(1.f / (1.f + expf(-s)));
    if (lane == 0) st4(rs, OFF_G + (unsigned)row * 4u, __float_as_uint(gg));
  }
}

__device__ __forceinline__ void out_unit(const Params& p, LAS unsigned char* lds, rsrc_t rs, int s, int cg, bool& dead) {
  const int tid = threadIdx.x, lane = tid & 63, w = __builtin_amdgcn_readfirstlane(tid >> 6);
  const int c16 = lane & 15, g = lane >> 4;
  const unsigned sb = OFF_STEP + (unsigned)s * STEP_BYTES;
  wait_flags(rs, OFF_UXF + (unsigned)(s * 32 + 4 * w + (lane & 3)) * 4u, dead);
  v4u ux[4][4];
#pragma unroll
  for (int ct = 0; ct < 4; ct++)
#pragma unroll
    for (int i = 0; i < 4; i++) ux[ct][i] = ld16(rs, OFF_UX + (unsigned)s * 1048576u + (unsigned)(((4 * cg + ct) * 32 + 4 * w + i) * 1024 + lane * 16));
  const int erow = tid >> 4, ecol = 64 * cg + 4 * (tid & 15);
  const v2u yb = ld8(rs, OFF_YB + (unsigned)((32 * s + erow) * 1024 + ecol * 2));
  const float gg = __uint_as_float(ld4(rs, OFF_G + (unsigned)(32 * s + erow) * 4u));
  wait_flags(rs, OFF_PXF + (unsigned)(s * 64 + 8 * w + (lane & 7)) * 4u, dead);
  const unsigned xb = OFF_PX + (unsigned)s * 81920u;
  v4u pa[2][5];
#pragma unroll
  for (int rt = 0; rt < 2; rt++) {
#pragma unroll
    for (int i = 0; i < 4; i++) pa[rt][i] = ld16(rs, xb + (unsigned)(((4 * w + i) * 2 + rt) * 1024 + lane * 16));
    pa[rt][4] = ld16(rs, xb + 65536u + (unsigned)((w * 2 + rt) * 1024 + lane * 16));
  }
  v4u um[4];
  {
    unsigned o[4];
#pragma unroll
    for (int ct = 0; ct < 4; ct++) o[ct] = sb + ST_UM + (unsigned)((((4 * cg + ct) * 32 + 4 * w + g) * 16 + c16) * 16);
    poll16<4>(rs, o, um, dead);
  }
#pragma unroll
  for (int rt = 0; rt < 2; rt++)
#pragma unroll
    for (int ct = 0; ct < 4; ct++) {
      f4 acc = {0.f, 0.f, 0.f, 0.f};
#pragma unroll
      for (int i = 0; i < 4; i++) acc = mfma16(pa[rt][i], ux[ct][i], acc);
      acc = mfma16(pa[rt][4], um[ct], acc);
      *(LAS f4*)(lds + w * 8192 + (rt * 4 + ct) * 1024 + lane * 16) = acc;
    }
  wgbar();
  {
    f4 t = *(const LAS f4*)(lds + tid * 16);
#pragma unroll
    for (int k = 1; k < 8; k++) t += *(const LAS f4*)(lds + k * 8192 + tid * 16);
    const int rt = w >> 2, ct = w & 3;
    LAS float* at = (LAS float*)(lds + 65536);
#pragma unroll
    for (int i = 0; i < 4; i++) at[(16 * rt + 4 * g + i) * 68 + 16 * ct + c16] = t[i];
  }
  wgbar();
  {
    f4 a = *(const LAS f4*)(lds + 65536 + (erow * 68 + 4 * (tid & 15)) * 4);
    const float og = rbf(1.f - gg);
    float b0 = __uint_as_float(yb.x << 16), b1 = __uint_as_float(yb.x & 0xFFFF0000u);
    float b2 = __uint_as_float(yb.y << 16), b3 = __uint_as_float(yb.y & 0xFFFF0000u);
    float o0 = rbf(gg * rbf(a[0])) + rbf(og * b0), o1 = rbf(gg * rbf(a[1])) + rbf(og * b1);
    float o2 = rbf(gg * rbf(a[2])) + rbf(og * b2), o3 = rbf(gg * rbf(a[3])) + rbf(og * b3);
    *(v2u*)(p.out + (size_t)(32 * s + erow) * 512 + ecol) = v2u{pk2(o0, o1), pk2(o2, o3)};
  }
}

__device__ __forceinline__ void role_spare(const Params& p, LAS unsigned char* lds, rsrc_t rs, int j) {
  const int tid = threadIdx.x, w = __builtin_amdgcn_readfirstlane(tid >> 6);
  bool dead = false;
  const v4u sv = {SENT, SENT, SENT, SENT};
  constexpr unsigned N0 = INIT0_STEPS * STEP_BYTES / 16, N1 = 64u * STEP_BYTES / 16;
  for (unsigned i = (unsigned)(j * 512 + tid); i < N0; i += NSPARE * 512) st16(rs, OFF_STEP + i * 16u, sv);
  drain(); wgbar();
  if (tid == 0) ctr_add(p, OFF_CTR + CTR_INIT0);
  for (unsigned i = N0 + (unsigned)(j * 512 + tid); i < N1; i += NSPARE * 512) st16(rs, OFF_STEP + i * 16u, sv);
  drain(); wgbar();
  if (tid == 0) ctr_add(p, OFF_CTR + CTR_INIT1);
  if (j < 128) ssm_unit(p, lds, rs, 4 * j); else gate_unit(p, rs, 64 * (j - 128));
  drain(); wgbar();
  if (tid == 0) ctr_add(p, OFF_CTR + CTR_SSM);
  if (w == 0) { wait_ctr(rs, OFF_CTR + CTR_SSM, NSPARE, dead); wait_ctr(rs, OFF_CTR + CTR_INIT1, NSPARE, dead); }
  wgbar();
  for (int u = j; u < 512; u += NSPARE) out_unit(p, lds, rs, u >> 3, u & 7, dead);
}

__global__ void __launch_bounds__(512, 2) fused_kernel(Params p) {
  extern __shared__ __attribute__((aligned(16))) unsigned char smem[];
  LAS unsigned char* lds = (LAS unsigned char*)smem;
  const rsrc_t rs = __builtin_amdgcn_make_buffer_rsrc((void*)p.ws, 0, (int)WS_BYTES, 0x00020000);
  const int b = blockIdx.x;
  if (b < 32) role_qk(p, lds, rs, b);
  else if (b < 64) role_vo(p, lds, rs, b - 32);
  else if (b < 96) role_r(p, lds, rs, b - 64);
  else role_spare(p, lds, rs, b - 96);
}

extern "C" void kernel_launch(void* const* d_in, const int* in_sizes, int n_in, void* d_out, int out_size,
                              void* d_ws, size_t ws_size, hipStream_t stream) {
  Params p;
  p.x = (const unsigned short*)d_in[0]; p.mem0 = (const unsigned short*)d_in[1];
  p.Wq = (const unsigned short*)d_in[2]; p.Wk = (const unsigned short*)d_in[3];
  p.Wv = (const unsigned short*)d_in[4]; p.Wo = (const unsigned short*)d_in[5];
  p.Are = (const unsigned short*)d_in[8]; p.Aim = (const unsigned short*)d_in[9];
  p.Bre = (const unsigned short*)d_in[10]; p.Bim = (const unsigned short*)d_in[11];
  p.Cre = (const unsigned short*)d_in[12]; p.Cim = (const unsigned short*)d_in[13];
  p.logdt = (const unsigned short*)d_in[14]; p.Dsk = (const unsigned short*)d_in[15];
  p.gw = (const unsigned short*)d_in[16];
  p.out = (unsigned short*)d_out; p.ws = (unsigned char*)d_ws;
  (void)hipFuncSetAttribute((const void*)fused_kernel, hipFuncAttributeMaxDynamicSharedMemorySize, LDS_BYTES);
  (void)hipMemsetAsync(d_ws, 0, CTR_BYTES, stream);
  fused_kernel<<<256, 512, LDS_BYTES, stream>>>(p);
}
```

```cpp
#include <hip/hip_runtime.h>
#include <hip/hip_bf16.h>
#include <stdint.h>

typedef unsigned v4u __attribute__((ext_vector_type(4)));
typedef unsigned v2u __attribute__((ext_vector_type(2)));
typedef float f4 __attribute__((ext_vector_type(4)));
typedef float f2 __attribute__((ext_vector_type(2)));
typedef __bf16 bf8 __attribute__((ext_vector_type(8)));
typedef __bf16 bf16x16 __attribute__((ext_vector_type(16)));
typedef __bf16 bf2 __attribute__((ext_vector_type(2)));
typedef short s4 __attribute__((ext_vector_type(4)));
typedef unsigned v8u __attribute__((ext_vector_type(8)));
#define LAS __attribute__((address_space(3)))
typedef __amdgpu_buffer_rsrc_t rsrc_t;

constexpr unsigned SENT = 0xFFFFFFFFu;
constexpr int AUX_SC1 = 16;
constexpr unsigned OFF_CTR = 0;
constexpr unsigned CTR_INIT0 = 0, CTR_INIT1 = 1024, CTR_SSM = 2048;
constexpr unsigned OFF_UXF = 4096;
constexpr unsigned OFF_PXF = 16384;
constexpr unsigned CTR_BYTES = 32768;
constexpr unsigned OFF_STEP = 65536;
constexpr unsigned ST_MEM = 0;
constexpr unsigned ST_P = 65536;
constexpr unsigned ST_UM = 65536 + 163840;
constexpr unsigned STEP_BYTES = 65536 + 163840 + 262144;
constexpr unsigned OFF_UX = OFF_STEP + 64u * STEP_BYTES;
constexpr unsigned OFF_PX = OFF_UX + 64u * 1048576u;
constexpr unsigned OFF_YB = OFF_PX + 64u * 81920u;
constexpr unsigned OFF_G = OFF_YB + 2097152u;
constexpr unsigned WS_BYTES = OFF_G + 8192u;
constexpr int INIT0_STEPS = 8;
constexpr int NSPARE = 160;
constexpr unsigned SPIN_CAP = 400000u;
constexpr int LDS_BYTES = 136 * 1024;

#define STAMPC(role, s, k) do {} while (0)
#define STAMP(role, s, k) do {} while (0)
struct Params {
  const unsigned short* x; const unsigned short* mem0; const unsigned short* Wq; const unsigned short* Wk;
  const unsigned short* Wv; const unsigned short* Wo; const unsigned short* Are; const unsigned short* Aim;
  const unsigned short* Bre; const unsigned short* Bim; const unsigned short* Cre; const unsigned short* Cim;
  const unsigned short* logdt; const unsigned short* Dsk; const unsigned short* gw;
  unsigned short* out; unsigned char* ws;
};

__device__ __forceinline__ float bf2f(unsigned short u) { return __uint_as_float((unsigned)u << 16); }
__device__ __forceinline__ unsigned pk2(float lo, float hi) {
  bf2 v; v[0] = (__bf16)lo; v[1] = (__bf16)hi; return __builtin_bit_cast(unsigned, v);
}
__device__ __forceinline__ float rbf(float f) { return (float)(__bf16)f; }
__device__ __forceinline__ unsigned short f2bfu(float f) { return __builtin_bit_cast(unsigned short, (__bf16)f); }
__device__ __forceinline__ unsigned san(unsigned u) { return u < 0xFFFFFFFEu ? u : 0xFFFFFFFEu; }
__device__ __forceinline__ v4u ld16(rsrc_t rs, unsigned off) { return __builtin_amdgcn_raw_buffer_load_b128(rs, off, 0, AUX_SC1); }
__device__ __forceinline__ unsigned ld4(rsrc_t rs, unsigned off) { return __builtin_amdgcn_raw_buffer_load_b32(rs, off, 0, AUX_SC1); }
__device__ __forceinline__ v2u ld8(rsrc_t rs, unsigned off) { return __builtin_amdgcn_raw_buffer_load_b64(rs, off, 0, AUX_SC1); }
__device__ __forceinline__ void st16(rsrc_t rs, unsigned off, v4u v) { __builtin_amdgcn_raw_buffer_store_b128(v, rs, off, 0, AUX_SC1); }
__device__ __forceinline__ void st8(rsrc_t rs, unsigned off, v2u v) { __builtin_amdgcn_raw_buffer_store_b64(v, rs, off, 0, AUX_SC1); }
__device__ __forceinline__ void st4(rsrc_t rs, unsigned off, unsigned v) { __builtin_amdgcn_raw_buffer_store_b32(v, rs, off, 0, AUX_SC1); }
__device__ __forceinline__ void cbar() { asm volatile("" ::: "memory"); }
__device__ __forceinline__ void drain() { asm volatile("s_waitcnt vmcnt(0)" ::: "memory"); }
__device__ __forceinline__ void wgbar() {
  asm volatile("s_waitcnt lgkmcnt(0)" ::: "memory"); __builtin_amdgcn_s_barrier(); asm volatile("" ::: "memory");
}
__device__ __forceinline__ unsigned mx4(v4u v) { return max(max(v.x, v.y), max(v.z, v.w)); }
__device__ __forceinline__ v4u lds16(const LAS unsigned char* p) { return *(const LAS v4u*)p; }
__device__ __forceinline__ v2u ldstr(const LAS unsigned char* p) {
  return __builtin_bit_cast(v2u, __builtin_amdgcn_ds_read_tr16_b64_v4i16((LAS s4*)p));
}
__device__ __forceinline__ bf8 asbf8(v4u v) { return __builtin_bit_cast(bf8, v); }
__device__ __forceinline__ f4 mfma16(v4u a, v4u b, f4 c) { return __builtin_amdgcn_mfma_f32_16x16x32_bf16(asbf8(a), asbf8(b), c, 0, 0, 0); }
__device__ __forceinline__ f4 smf(v4u a, v4u b0, v4u b1, f4 c, int idx) {
  v8u b = {b0.x, b0.y, b0.z, b0.w, b1.x, b1.y, b1.z, b1.w};
  return __builtin_amdgcn_smfmac_f32_16x16x64_bf16(asbf8(a), __builtin_bit_cast(bf16x16, b), c, idx, 0, 0);
}
__device__ __forceinline__ float x32(float v) {
  unsigned u = __float_as_uint(v);
  auto r = __builtin_amdgcn_permlane32_swap(u, u, false, false);
  return __uint_as_float((threadIdx.x & 32) ? r[0] : r[1]);
}
__device__ __forceinline__ float add32(float v) { unsigned u = __float_as_uint(v); auto r = __builtin_amdgcn_permlane32_swap(u, u, false, false); return __uint_as_float(r[0]) + __uint_as_float(r[1]); }
__device__ __forceinline__ float max32(float v) { unsigned u = __float_as_uint(v); auto r = __builtin_amdgcn_permlane32_swap(u, u, false, false); return fmaxf(__uint_as_float(r[0]), __uint_as_float(r[1])); }
__device__ __forceinline__ float add16(float v) { unsigned u = __float_as_uint(v); auto r = __builtin_amdgcn_permlane16_swap(u, u, false, false); return __uint_as_float(r[0]) + __uint_as_float(r[1]); }
__device__ __forceinline__ float max16(float v) { unsigned u = __float_as_uint(v); auto r = __builtin_amdgcn_permlane16_swap(u, u, false, false); return fmaxf(__uint_as_float(r[0]), __uint_as_float(r[1])); }
__device__ __forceinline__ float addror8(float v) {
  int r = __builtin_amdgcn_update_dpp(0, __float_as_int(v), 0x128, 0xF, 0xF, false);
  return v + __int_as_float(r);
}
__device__ __forceinline__ float fold2(float a, float b) {
  auto r = __builtin_amdgcn_permlane32_swap(__float_as_uint(a), __float_as_uint(b), false, false);
  return __uint_as_float(r[0]) + __uint_as_float(r[1]);
}

__device__ __forceinline__ void wait_ctr(rsrc_t rs, unsigned off, bool& dead) {
  unsigned spins = dead ? SPIN_CAP : 0;
  const unsigned lane = threadIdx.x & 63;
  while (true) {
    cbar();
    v4u v = ld16(rs, off + (lane < NSPARE / 4 ? lane : 0u) * 16u);
    if (__all(min(min(v.x, v.y), min(v.z, v.w)) != 0u)) { cbar(); return; }
    if (++spins > SPIN_CAP) { dead = true; return; }
    __builtin_amdgcn_s_sleep(8);
  }
}
template <int SL>
__device__ __forceinline__ void wait_flags(rsrc_t rs, unsigned off, bool& dead) {
  unsigned spins = dead ? SPIN_CAP : 0;
  while (true) {
    cbar();
    unsigned v = ld4(rs, off);
    if (__all(v != 0u)) { cbar(); return; }
    if (++spins > SPIN_CAP) { dead = true; return; }
    __builtin_amdgcn_s_sleep(SL);
  }
}
template <int C>
__device__ __forceinline__ void poll16(rsrc_t rs, const unsigned (&off)[C], v4u (&v)[C], bool& dead) {
  unsigned spins = dead ? SPIN_CAP : 0;
  while (true) {
    cbar();
#pragma unroll
    for (int c = 0; c < C; c++) v[c] = ld16(rs, off[c]);
    unsigned m = 0;
#pragma unroll
    for (int c = 0; c < C; c++) m = max(m, mx4(v[c]));
    if (__all(m != SENT)) return;
    if (++spins > SPIN_CAP) { dead = true; return; }
  }
}

template <int NCT>
__device__ __forceinline__ void wload_issue(const unsigned short* src, int rowstride, int lane, v4u (&t)[NCT * 2]) {
  constexpr int LPR = NCT * 2;
  constexpr int RPI = 64 / LPR;
#pragma unroll
  for (int i = 0; i < NCT * 2; i++) {
    int row = i * RPI + lane / LPR, pc = lane % LPR;
    t[i] = *(const v4u*)(src + (size_t)row * rowstride + pc * 8);
  }
}
template <int NCT>
__device__ __forceinline__ void wload_finish(LAS unsigned char* slab, int lane, const v4u (&t)[NCT * 2], v4u (&f)[NCT][2]) {
  constexpr int LPR = NCT * 2, RPI = 64 / LPR, RB = NCT * 32;
#pragma unroll
  for (int i = 0; i < NCT * 2; i++) {
    int row = i * RPI + lane / LPR, pc = lane % LPR;
    *(LAS v4u*)(slab + row * RB + pc * 16) = t[i];
  }
  asm volatile("s_waitcnt lgkmcnt(0)" ::: "memory");
  int gb = lane >> 4, q = (lane >> 2) & 3, p = lane & 3;
#pragma unroll
  for (int ct = 0; ct < NCT; ct++)
#pragma unroll
    for (int hf = 0; hf < 2; hf++) {
      v2u a = ldstr(slab + (32 * hf + 8 * gb + q) * RB + ct * 32 + p * 8);
      v2u b = ldstr(slab + (32 * hf + 8 * gb + 4 + q) * RB + ct * 32 + p * 8);
      f[ct][hf] = v4u{a.x, a.y, b.x, b.y};
    }
  asm volatile("s_waitcnt lgkmcnt(0)" ::: "memory");
}

constexpr int WROW = 272;
__device__ __forceinline__ v4u wrow_issue(const unsigned short* src, int row0, int tid) {
  return *(const v4u*)(src + (size_t)(row0 + (tid >> 4)) * 4096 + (tid & 15) * 8);
}
__device__ __forceinline__ void wrow_store(LAS unsigned char* img, int row0, int tid, v4u v) {
  *(LAS v4u*)(img + (row0 + (tid >> 4)) * WROW + (tid & 15) * 16) = v;
}
__device__ __forceinline__ v4u wrow_frag(const LAS unsigned char* img, int row0, int colbyte, int lane) {
  const int gb = lane >> 4, q = (lane >> 2) & 3, p = lane & 3;
  v2u a = ldstr(img + (row0 + 8 * gb + q) * WROW + colbyte + p * 8);
  v2u b = ldstr(img + (row0 + 8 * gb + 4 + q) * WROW + colbyte + p * 8);
  return v4u{a.x, a.y, b.x, b.y};
}
constexpr int L_XS = 0;
constexpr int L_MEM = 65536;
constexpr int L_QKT = 73728;
constexpr int L_QXT = 81920;
constexpr int L_KXT = 90112;
constexpr int L_KXP = 98304;
constexpr int L_SLAB = 0;

__device__ __forceinline__ void x_dma(const Params& p, LAS unsigned char* lds, int s, int w, int lane) {
#pragma unroll
  for (int i = 0; i < 4; i++) {
    const int r = 4 * w + i;
    const unsigned short* src = p.x + (size_t)(s * 32 + r) * 512 + (lane ^ (r & 15)) * 8;
    const unsigned dst = __builtin_amdgcn_readfirstlane((unsigned)(uintptr_t)(lds + L_XS + (s & 1) * 32768 + r * 1024));
    unsigned keep;
    asm volatile("s_mov_b32 %0, m0\n\ts_mov_b32 m0, %2\n\ts_nop 0\n\tglobal_load_lds_dwordx4 %1, off\n\ts_mov_b32 m0, %0"
                 : "=&s"(keep) : "v"(src), "s"(dst) : "memory");
  }
}
__device__ __forceinline__ v4u x_frag(const LAS unsigned char* lds, int s, int rt, int ks, int c16, int g) {
  return lds16(lds + ((s & 1) * 32768 + c16 * 1024 + ((4 * (ks & 3) + g) ^ c16) * 16) + (L_XS + rt * 16384 + (ks >> 2) * 256));
}
__device__ __forceinline__ v4u mem0_chunk(const Params& p, int w, int lane) {
  int r = lane & 15, ga = lane >> 4;
  const unsigned* m = (const unsigned*)(p.mem0 + (r & 7) * 512 + 64 * w + 16 * ga + 2 * (r >> 3));
  return v4u{m[0], m[2], m[4], m[6]};
}

constexpr float SCL2 = 0.08838834764831845f * 1.4426950408889634f;

__device__ __forceinline__ void role_qk(const Params& p, LAS unsigned char* lds, rsrc_t rs, int h) {
  const int tid = threadIdx.x, lane = tid & 63, w = __builtin_amdgcn_readfirstlane(tid >> 6);
  const int c16 = lane & 15, g = lane >> 4, tq = (lane >> 2) & 3, tp = lane & 3;
  const int idx = (c16 < 8) ? 0x4444 : 0xEEEE;
  const unsigned cp = (unsigned)(blockIdx.x & 7);
  bool dead = false;
  v4u Wf[8][2][2];
  {
    v4u t[2][2][8];
#pragma unroll
    for (int r = 0; r < 2; r++)
#pragma unroll
      for (int i = 0; i < 8; i++) {
        t[r][0][i] = wrow_issue(p.Wq + h * 128, 256 * r + 32 * i, tid);
        t[r][1][i] = wrow_issue(p.Wk + h * 128, 256 * r + 32 * i, tid);
      }
#pragma unroll
    for (int r = 0; r < 2; r++) {
#pragma unroll
      for (int i = 0; i < 8; i++) { wrow_store(lds, 32 * i, tid, t[r][0][i]); wrow_store(lds + 256 * WROW, 32 * i, tid, t[r][1][i]); }
      wgbar();
#pragma unroll
      for (int k = 0; k < 4; k++)
#pragma unroll
        for (int ct = 0; ct < 2; ct++)
#pragma unroll
          for (int hf = 0; hf < 2; hf++)
            Wf[4 * r + k][ct][hf] = wrow_frag(lds + (w >> 2) * 256 * WROW, 64 * k + 32 * hf, (w & 3) * 64 + ct * 32, lane);
      wgbar();
    }
  }
  x_dma(p, lds, 0, w, lane);
  if (w == 7) wait_ctr(rs, OFF_CTR + CTR_INIT0, dead);
  drain(); wgbar();

  v4u fr[8];
  f4 sxx[2];
  for (int s = 0; s < 64; s++) {
    STAMP(blockIdx.x, s, 0);
    {
      f4 acc[2][2];
#pragma unroll
      for (int rt = 0; rt < 2; rt++)
#pragma unroll
        for (int ct = 0; ct < 2; ct++) acc[rt][ct] = f4{0.f, 0.f, 0.f, 0.f};
      if (s < 63) x_dma(p, lds, s + 1, w, lane);
      v4u a[2][2][2];
#pragma unroll
      for (int k = 0; k < 2; k++)
#pragma unroll
        for (int rt = 0; rt < 2; rt++) a[0][k][rt] = x_frag(lds, s, rt, k, c16, g);
#pragma unroll
      for (int kk = 0; kk < 8; kk++) {
        if (kk < 7) {
#pragma unroll
          for (int k = 0; k < 2; k++)
#pragma unroll
            for (int rt = 0; rt < 2; rt++) a[(kk + 1) & 1][k][rt] = x_frag(lds, s, rt, 2 * kk + 2 + k, c16, g);
        }
        __builtin_amdgcn_sched_barrier(0);
#pragma unroll
        for (int k = 0; k < 2; k++)
#pragma unroll
          for (int rt = 0; rt < 2; rt++)
#pragma unroll
            for (int ct = 0; ct < 2; ct++) acc[rt][ct] = mfma16(a[kk & 1][k][rt], Wf[kk][ct][k], acc[rt][ct]);
        __builtin_amdgcn_sched_barrier(0);
      }
#pragma unroll
      for (int rt = 0; rt < 2; rt++)
#pragma unroll
        for (int ct = 0; ct < 2; ct++) {
          int e = 32 * (w & 3) + 16 * ct + c16;
          unsigned p0 = pk2(acc[rt][ct][0], acc[rt][ct][1]), p1 = pk2(acc[rt][ct][2], acc[rt][ct][3]);
          *(LAS v2u*)(lds + (w < 4 ? L_QXT : L_KXT) + e * 64 + (16 * rt + 4 * g) * 2) = v2u{p0, p1};
          if (w >= 4) {
            int pos = (g & 1) * 16 + (g >> 1) * 8 + 2 * rt;
            *(LAS unsigned*)(lds + L_KXP + e * 64 + pos * 2) = p0;
            *(LAS unsigned*)(lds + L_KXP + e * 64 + (pos + 4) * 2) = p1;
          }
        }
    }
    wgbar();
    if (w < 4) {
#pragma unroll
      for (int tau = 0; tau < 2; tau++)
#pragma unroll
        for (int ks = 0; ks < 4; ks++) {
          v2u a = ldstr(lds + L_KXP + (32 * ks + 8 * g + tq) * 64 + tau * 32 + tp * 8);
          v2u b = ldstr(lds + L_KXP + (32 * ks + 8 * g + 4 + tq) * 64 + tau * 32 + tp * 8);
          fr[tau * 4 + ks] = v4u{a.x, a.y, b.x, b.y};
        }
    } else if (w < 6) {
      const int rt = w - 4;
      sxx[0] = f4{0.f, 0.f, 0.f, 0.f}; sxx[1] = sxx[0];
#pragma unroll
      for (int ks = 0; ks < 4; ks++) {
        v2u a = ldstr(lds + L_QXT + (32 * ks + 8 * g + tq) * 64 + rt * 32 + tp * 8);
        v2u b = ldstr(lds + L_QXT + (32 * ks + 8 * g + 4 + tq) * 64 + rt * 32 + tp * 8);
        fr[ks] = v4u{a.x, a.y, b.x, b.y};
#pragma unroll
        for (int tau = 0; tau < 2; tau++) {
          v2u c = ldstr(lds + L_KXT + (32 * ks + 8 * g + tq) * 64 + tau * 32 + tp * 8);
          v2u d = ldstr(lds + L_KXT + (32 * ks + 8 * g + 4 + tq) * 64 + tau * 32 + tp * 8);
          sxx[tau] = mfma16(v4u{c.x, c.y, d.x, d.y}, fr[ks], sxx[tau]);
        }
      }
    } else if (w == 7 && s == INIT0_STEPS - 1) {
      wait_ctr(rs, OFF_CTR + CTR_INIT1, dead);
    }
    const unsigned sb = OFF_STEP + (unsigned)s * STEP_BYTES;
    STAMP(blockIdx.x, s, 1);
    {
      v4u m[1];
      if (s == 0) m[0] = mem0_chunk(p, w, lane);
      else { const unsigned o[1] = {sb + ST_MEM + cp * 8192u + (unsigned)tid * 16u}; poll16<1>(rs, o, m, dead); }
      STAMP(blockIdx.x, s, 2);
      *(LAS v4u*)(lds + L_MEM + tid * 16) = m[0];
      drain();
    }
    wgbar();
    STAMP(blockIdx.x, s, 3);
    {
      f4 acc[2] = {f4{0.f, 0.f, 0.f, 0.f}, f4{0.f, 0.f, 0.f, 0.f}};
      v4u a[8];
#pragma unroll
      for (int t = 0; t < 8; t++) a[t] = lds16(lds + L_MEM + t * 1024 + lane * 16);
      __builtin_amdgcn_sched_barrier(0);
#pragma unroll
      for (int t = 0; t < 8; t++)
#pragma unroll
        for (int ct = 0; ct < 2; ct++) acc[ct] = smf(a[t], Wf[t][ct][0], Wf[t][ct][1], acc[ct], idx);
#pragma unroll
      for (int ct = 0; ct < 2; ct++)
        *(LAS v2u*)(lds + L_QKT + (32 * w + 16 * ct + c16) * 32 + g * 8) =
            v2u{pk2(acc[ct][0], acc[ct][1]), pk2(acc[ct][2], acc[ct][3])};
    }
    wgbar();
    STAMP(blockIdx.x, s, 4);
    if (w < 4) {
      if (s < 63) {
        __builtin_amdgcn_s_setprio(3);
        f4 sx0 = {0.f, 0.f, 0.f, 0.f}, sx1 = sx0, sm = sx0;
#pragma unroll
        for (int ks = 0; ks < 4; ks++) {
          v2u a = ldstr(lds + L_QKT + (32 * ks + 8 * g + tq) * 32 + tp * 8);
          v2u b = ldstr(lds + L_QKT + (32 * ks + 8 * g + 4 + tq) * 32 + tp * 8);
          v2u c = ldstr(lds + L_QKT + (128 + 32 * ks + 8 * g + tq) * 32 + tp * 8);
          v2u d = ldstr(lds + L_QKT + (128 + 32 * ks + 8 * g + 4 + tq) * 32 + tp * 8);
          v4u qf = {a.x, a.y, b.x, b.y};
          sx0 = mfma16(fr[ks], qf, sx0);
          sx1 = mfma16(fr[4 + ks], qf, sx1);
          sm = mfma16(v4u{c.x, c.y, d.x, d.y}, qf, sm);
        }
        float v[12];
#pragma unroll
        for (int i = 0; i < 4; i++) { v[i] = addror8(sx0[i]); v[4 + i] = addror8(sx1[i]); v[8 + i] = add32(addror8(sm[i])); }
        float mx = v[0];
#pragma unroll
        for (int i = 1; i < 12; i++) mx = fmaxf(mx, v[i]);
        mx = max32(max16(mx));
        float sum = 0.f, summ = 0.f;
#pragma unroll
        for (int i = 0; i < 12; i++) { v[i] = __builtin_amdgcn_exp2f((v[i] - mx) * SCL2); if (i < 8) sum += v[i]; else summ += v[i]; }
        sum += 0.5f * summ;
        sum = add32(add16(sum));
        float inv = __builtin_amdgcn_rcpf(sum);
        v4u px = {san(pk2(v[0] * inv, v[1] * inv)), san(pk2(v[2] * inv, v[3] * inv)), san(pk2(v[4] * inv, v[5] * inv)), san(pk2(v[6] * inv, v[7] * inv))};
        v2u pm = {san(pk2(v[8] * inv, v[9] * inv)), san(pk2(v[10] * inv, v[11] * inv))};
        const unsigned pb = sb + ST_P + (unsigned)(2 * w + (c16 >> 3)) * 20480u;
        st16(rs, pb + (unsigned)(h * 512 + (g * 8 + (c16 & 7)) * 16), px);
        if (g < 2) st8(rs, pb + 16384u + (unsigned)((((h >> 1) * 16) + g * 8 + (c16 & 7)) * 16 + (h & 1) * 8), pm);
        __builtin_amdgcn_s_setprio(0);
      }
    } else if (w < 6) {
      const int rt = w - 4;
      if (lane == 0 && s > 0) st4(rs, OFF_PXF + (unsigned)((s - 1) * 64 + h * 2 + rt) * 4u, 1u);
      f4 sm = {0.f, 0.f, 0.f, 0.f};
#pragma unroll
      for (int ks = 0; ks < 4; ks++) {
        v2u c = ldstr(lds + L_QKT + (128 + 32 * ks + 8 * g + tq) * 32 + tp * 8);
        v2u d = ldstr(lds + L_QKT + (128 + 32 * ks + 8 * g + 4 + tq) * 32 + tp * 8);
        sm = mfma16(v4u{c.x, c.y, d.x, d.y}, fr[ks], sm);
      }
      float v[12];
      const int R = 16 * rt + c16;
#pragma unroll
      for (int i = 0; i < 4; i++) {
        v[i] = (4 * g + i <= R) ? sxx[0][i] : -3.0e38f;
        v[4 + i] = (16 + 4 * g + i <= R) ? sxx[1][i] : -3.0e38f;
        v[8 + i] = add32(sm[i]);
      }
      float mx = v[0];
#pragma unroll
      for (int i = 1; i < 12; i++) mx = fmaxf(mx, v[i]);
      mx = max32(max16(mx));
      float sum = 0.f, summ = 0.f;
#pragma unroll
      for (int i = 0; i < 12; i++) {
        float e = __builtin_amdgcn_exp2f((v[i] - mx) * SCL2);
        if (i < 4) e = (4 * g + i <= R) ? e : 0.f; else if (i < 8) e = (12 + 4 * g + i <= R) ? e : 0.f;
        v[i] = e; if (i < 8) sum += e; else summ += e;
      }
      sum += 0.5f * summ;
      sum = add32(add16(sum));
      float inv = __builtin_amdgcn_rcpf(sum);
      v4u px = {pk2(v[0] * inv, v[1] * inv), pk2(v[2] * inv, v[3] * inv), pk2(v[4] * inv, v[5] * inv), pk2(v[6] * inv, v[7] * inv)};
      unsigned pm = (g < 2) ? pk2(v[8] * inv, v[9] * inv) : pk2(v[10] * inv, v[11] * inv);
      const unsigned xb = OFF_PX + (unsigned)s * 81920u;
      st16(rs, xb + (unsigned)((h * 2 + rt) * 1024 + lane * 16), px);
      st4(rs, xb + 65536u + (unsigned)((((h >> 2) * 2 + rt) * 64 + (h & 3) * 16 + c16) * 16 + g * 4), pm);
      if (s == 63) { drain(); if (lane == 0) st4(rs, OFF_PXF + (unsigned)(s * 64 + h * 2 + rt) * 4u, 1u); }
    }
    STAMP(blockIdx.x, s, 5); STAMPC(blockIdx.x, s, 6);
    __builtin_amdgcn_s_barrier();
  }
}

__device__ __forceinline__ void role_vo(const Params& p, LAS unsigned char* lds, rsrc_t rs, int h) {
  const int tid = threadIdx.x, lane = tid & 63, w = __builtin_amdgcn_readfirstlane(tid >> 6);
  const int c16 = lane & 15, g = lane >> 4, tq = (lane >> 2) & 3, tp = lane & 3;
  const int idx = (c16 < 8) ? 0x4444 : 0xEEEE;
  const unsigned cp = (unsigned)(blockIdx.x & 7);
  bool dead = false;
  v4u Vf[8][1][2], Of[2][4][2];
  {
    const unsigned short* so = p.Wo + (size_t)h * 65536 + 64 * w;
    v4u t[16], t2[2][8];
#pragma unroll
    for (int i = 0; i < 16; i++) t[i] = wrow_issue(p.Wv + h * 128, 32 * i, tid);
#pragma unroll
    for (int k = 0; k < 2; k++) wload_issue<4>(so + (size_t)k * 64 * 512, 512, lane, t2[k]);
#pragma unroll
    for (int i = 0; i < 16; i++) wrow_store(lds, 32 * i, tid, t[i]);
    wgbar();
#pragma unroll
    for (int k = 0; k < 8; k++)
#pragma unroll
      for (int hf = 0; hf < 2; hf++) Vf[k][0][hf] = wrow_frag(lds, 64 * k + 32 * hf, w * 32, lane);
    wgbar();
#pragma unroll
    for (int k = 0; k < 2; k++) wload_finish<4>(lds + L_SLAB + w * 8192, lane, t2[k], Of[k]);
    wgbar();
  }
  x_dma(p, lds, 0, w, lane);
  drain(); wgbar();
  for (int s = -1; s < 64; s++) {
    const int sx = s + 1;
    if (sx < 64) {
      STAMP(blockIdx.x, sx, 0);
      {
        f4 acc[2] = {f4{0.f, 0.f, 0.f, 0.f}, f4{0.f, 0.f, 0.f, 0.f}};
        if (sx < 63) x_dma(p, lds, sx + 1, w, lane);
        v4u a[3][2][2];
#pragma unroll
        for (int q = 0; q < 2; q++)
#pragma unroll
          for (int k = 0; k < 2; k++)
#pragma unroll
            for (int rt = 0; rt < 2; rt++) a[q][k][rt] = x_frag(lds, sx, rt, 2 * q + k, c16, g);
#pragma unroll
        for (int kk = 0; kk < 8; kk++) {
          if (kk < 6) {
#pragma unroll
            for (int k = 0; k < 2; k++)
#pragma unroll
              for (int rt = 0; rt < 2; rt++) a[(kk + 2) % 3][k][rt] = x_frag(lds, sx, rt, 2 * kk + 4 + k, c16, g);
          }
          __builtin_amdgcn_sched_barrier(0);
#pragma unroll
          for (int k = 0; k < 2; k++)
#pragma unroll
            for (int rt = 0; rt < 2; rt++) acc[rt] = mfma16(a[kk % 3][k][rt], Vf[kk][0][k], acc[rt]);
          __builtin_amdgcn_sched_barrier(0);
        }
#pragma unroll
        for (int rt = 0; rt < 2; rt++)
          *(LAS v2u*)(lds + L_QXT + (16 * w + c16) * 64 + (16 * rt + 4 * g) * 2) = v2u{pk2(acc[rt][0], acc[rt][1]), pk2(acc[rt][2], acc[rt][3])};
      }
      wgbar();
      {
        f4 acc[2][4];
#pragma unroll
        for (int rt = 0; rt < 2; rt++)
#pragma unroll
          for (int ct = 0; ct < 4; ct++) acc[rt][ct] = f4{0.f, 0.f, 0.f, 0.f};
        v4u af[4][2];
#pragma unroll
        for (int ks = 0; ks < 4; ks++)
#pragma unroll
          for (int rt = 0; rt < 2; rt++) {
            v2u a = ldstr(lds + L_QXT + (32 * ks + 8 * g + tq) * 64 + rt * 32 + tp * 8);
            v2u b = ldstr(lds + L_QXT + (32 * ks + 8 * g + 4 + tq) * 64 + rt * 32 + tp * 8);
            af[ks][rt] = v4u{a.x, a.y, b.x, b.y};
          }
        __builtin_amdgcn_sched_barrier(0);
#pragma unroll
        for (int ks = 0; ks < 4; ks++)
#pragma unroll
          for (int rt = 0; rt < 2; rt++)
#pragma unroll
            for (int ct = 0; ct < 4; ct++) acc[rt][ct] = mfma16(af[ks][rt], Of[ks >> 1][ct][ks & 1], acc[rt][ct]);
#pragma unroll
        for (int ct = 0; ct < 4; ct++) {
          v4u u = {pk2(acc[0][ct][0], acc[0][ct][1]), pk2(acc[0][ct][2], acc[0][ct][3]), pk2(acc[1][ct][0], acc[1][ct][1]), pk2(acc[1][ct][2], acc[1][ct][3])};
          st16(rs, OFF_UX + (unsigned)sx * 1048576u + (unsigned)(((4 * w + ct) * 32 + h) * 1024 + lane * 16), u);
        }
      }
    }
    if (s < 0) {
      if (w == 7) wait_ctr(rs, OFF_CTR + CTR_INIT0, dead);
      drain(); wgbar();
      if (tid == 0) st4(rs, OFF_UXF + (unsigned)h * 4u, 1u);
      continue;
    }
    if (w == 7 && s == INIT0_STEPS - 1) wait_ctr(rs, OFF_CTR + CTR_INIT1, dead);
    STAMP(blockIdx.x, s, 1);
    const unsigned sb = OFF_STEP + (unsigned)s * STEP_BYTES;
    {
      v4u m[1];
      if (s == 0) m[0] = mem0_chunk(p, w, lane);
      else { const unsigned o[1] = {sb + ST_MEM + cp * 8192u + (unsigned)tid * 16u}; poll16<1>(rs, o, m, dead); }
      STAMP(blockIdx.x, s, 2);
      *(LAS v4u*)(lds + L_MEM + tid * 16) = m[0];
      drain();
    }
    wgbar();
    if (tid == 64 && sx < 64) st4(rs, OFF_UXF + (unsigned)(sx * 32 + h) * 4u, 1u);
    {
      f4 a0 = {0.f, 0.f, 0.f, 0.f}, a1 = a0;
      v4u a[8];
#pragma unroll
      for (int t = 0; t < 8; t++) a[t] = lds16(lds + L_MEM + t * 1024 + lane * 16);
      __builtin_amdgcn_sched_barrier(0);
#pragma unroll
      for (int t = 0; t < 8; t += 2) {
        a0 = smf(a[t], Vf[t][0][0], Vf[t][0][1], a0, idx);
        a1 = smf(a[t + 1], Vf[t + 1][0][0], Vf[t + 1][0][1], a1, idx);
      }
      a0 += a1;
      float f0 = fold2(a0[0], a0[1]), f1 = fold2(a0[2], a0[3]);
      const int gp = g & 1, up = g >> 1;
      const int hf = (c16 >> 1) & 1, j = 2 * (c16 >> 2) + (c16 & 1);
      LAS unsigned char* b = lds + L_QKT + (w * 16 + hf * 8 + 2 * gp + up) * 16 + j * 2;
      *(LAS unsigned short*)(b) = f2bfu(f0);
      *(LAS unsigned short*)(b + 64) = f2bfu(f1);
    }
    wgbar();
    {
      v4u a0 = lds16(lds + L_QKT + lane * 16), a1 = lds16(lds + L_QKT + 1024 + lane * 16);
      f4 acc[4];
#pragma unroll
      for (int ct = 0; ct < 4; ct++) {
        acc[ct] = smf(a0, Of[0][ct][0], Of[0][ct][1], f4{0.f, 0.f, 0.f, 0.f}, idx);
        acc[ct] = smf(a1, Of[1][ct][0], Of[1][ct][1], acc[ct], idx);
      }
#pragma unroll
      for (int ct = 0; ct < 4; ct += 2) {
        float f[4];
#pragma unroll
        for (int i = 0; i < 4; i++) f[i] = fold2(acc[ct][i], acc[ct + 1][i]);
        v2u u = {san(pk2(f[0], f[1])), san(pk2(f[2], f[3]))};
        st8(rs, sb + ST_UM + (unsigned)((((4 * w + ct + (g >> 1)) * 32 + h) * 16 + c16) * 16 + (g & 1) * 8), u);
      }
    }
    STAMP(blockIdx.x, s, 5);
  }
}

__device__ __forceinline__ void warm_weights(const Params& p, int cls, int q) {
  unsigned acc = 0;
  for (int i = q * 512 + (int)threadIdx.x; i < 16384; i += 24 * 512) {
    const int line = i & 1, row = (i >> 1) & 511, m = (i >> 10) & 3, hh = cls + 8 * (i >> 12);
    const unsigned short* a = m == 3 ? p.Wo + (size_t)hh * 65536 + (row * 2 + line) * 64
                                     : (m == 0 ? p.Wq : m == 1 ? p.Wk : p.Wv) + (size_t)row * 4096 + hh * 128 + line * 64;
    acc += *(const unsigned*)a;
  }
  asm volatile("" :: "v"(acc));
}
__device__ __forceinline__ void role_r(const Params& p, LAS unsigned char* lds, rsrc_t rs, int c) {
  const int tid = threadIdx.x, lane = tid & 63, w = __builtin_amdgcn_readfirstlane(tid >> 6);
  const int c16 = lane & 15, g = lane >> 4;
  const int idx = (c16 < 8) ? 0x4444 : 0xEEEE;
  const unsigned cp = (unsigned)(blockIdx.x & 7);
  bool dead = false;
  warm_weights(p, c & 7, c >> 3);
  if (w == 7) wait_ctr(rs, OFF_CTR + CTR_INIT0, dead);
  wgbar();
  for (int s = 0; s < 63; s++) {
    const unsigned sb = OFF_STEP + (unsigned)s * STEP_BYTES;
    if (s == INIT0_STEPS - 2) wait_ctr(rs, OFF_CTR + CTR_INIT1, dead);
    STAMP(blockIdx.x, s, 0);
    wait_flags<4>(rs, OFF_UXF + (unsigned)(s * 32 + 4 * w + (lane & 3)) * 4u, dead);
    STAMP(blockIdx.x, s, 1);
    v4u ux[4];
#pragma unroll
    for (int i = 0; i < 4; i++) ux[i] = ld16(rs, OFF_UX + (unsigned)s * 1048576u + (unsigned)((c * 32 + 4 * w + i) * 1024 + lane * 16));
    f4 acc = {0.f, 0.f, 0.f, 0.f};
    const unsigned pb = sb + ST_P + cp * 20480u;
    if (w < 4) {
      v4u um[2];
      { const unsigned o[2] = {sb + ST_UM + (unsigned)(((c * 32 + 8 * w + g) * 16 + c16) * 16), sb + ST_UM + (unsigned)(((c * 32 + 8 * w + 4 + g) * 16 + c16) * 16)};
        poll16<2>(rs, o, um, dead); }
      STAMP(blockIdx.x, s, 2);
      v4u pa[3];
      { const unsigned o[3] = {pb + (unsigned)(2 * w * 1024 + lane * 16), pb + (unsigned)((2 * w + 1) * 1024 + lane * 16), pb + 16384u + (unsigned)(w * 1024 + lane * 16)};
        poll16<3>(rs, o, pa, dead); }
      acc = smf(pa[0], ux[0], ux[1], acc, idx);
      acc = smf(pa[1], ux[2], ux[3], acc, idx);
      acc = smf(pa[2], um[0], um[1], acc, idx);
    } else {
      v4u pa[2];
      { const unsigned o[2] = {pb + (unsigned)(2 * w * 1024 + lane * 16), pb + (unsigned)((2 * w + 1) * 1024 + lane * 16)};
        poll16<2>(rs, o, pa, dead); }
      acc = smf(pa[0], ux[0], ux[1], acc, idx);
      acc = smf(pa[1], ux[2], ux[3], acc, idx);
    }
    STAMP(blockIdx.x, s, 3);
    {
      float f0 = fold2(acc[0], acc[1]), f1 = fold2(acc[2], acc[3]);
      LAS float* red = (LAS float*)(lds + (s & 1) * 4096) + w * 128 + (4 * (g & 1) + (g >> 1)) * 16 + c16;
      red[0] = f0; red[32] = f1;
    }
    wgbar();
    {
      const int r = lane >> 2, dq = lane & 3;
      const LAS f2* red = (const LAS f2*)((LAS float*)(lds + (s & 1) * 4096) + (r & 7) * 16 + 4 * dq + 2 * (r >> 3));
      f2 a = red[0], b = red[64], c2 = red[128], d = red[192], e = red[256], f = red[320], gg = red[384], hh = red[448];
      f2 t = ((a + b) + (c2 + d)) + ((e + f) + (gg + hh));
      st4(rs, sb + STEP_BYTES + ST_MEM + (unsigned)(w * 8192 + c * 256 + lane * 4), san(pk2(t[0], t[1])));
    }
    STAMP(blockIdx.x, s, 5);
  }
}

constexpr int S_UC = 4288;
constexpr int S_RV = 4640;
constexpr int S_CH = 4 * S_UC + 2 * S_RV;
constexpr int S_KD = 4 * S_CH;
constexpr int LAGMAX = 2064;

__device__ __forceinline__ void cexp2(float a2, float brev, float L, float& re, float& im) {
  float mag = __builtin_amdgcn_exp2f(a2 * L);
  float ph = __builtin_amdgcn_fractf(brev * L);
  re = mag * __builtin_amdgcn_cosf(ph); im = mag * __builtin_amdgcn_sinf(ph);
}

__device__ __forceinline__ void ssm_unit(const Params& p, LAS unsigned char* lds, rsrc_t rs, int d0) {
  const int tid = threadIdx.x, lane = tid & 63, w = __builtin_amdgcn_readfirstlane(tid >> 6);
  const int c16 = lane & 15, g = lane >> 4, ch = w >> 1, hw = w & 1, d = d0 + ch;
  for (int i = tid; i < 4 * S_CH / 16; i += 512) *(LAS v4u*)(lds + i * 16) = v4u{0u, 0u, 0u, 0u};
  v2u xv[4];
#pragma unroll
  for (int i = 0; i < 4; i++) xv[i] = *(const v2u*)(p.x + (size_t)(i * 512 + tid) * 512 + d0);
  wgbar();
#pragma unroll
  for (int i = 0; i < 4; i++) {
    const int l = i * 512 + tid;
#pragma unroll
    for (int c = 0; c < 4; c++) {
      unsigned short v = (unsigned short)((c & 2 ? xv[i].y : xv[i].x) >> (16 * (c & 1)));
#pragma unroll
      for (int r = 0; r < 4; r++) *(LAS unsigned short*)(lds + c * S_CH + r * S_UC + (l + 32 - r) * 2) = v;
    }
  }
  {
    const float dt = rbf(expf(bf2f(p.logdt[d])));
    float L1[3];
    L1[0] = (float)((c16 & 7) + 128 * (c16 >> 3));
    L1[1] = (c16 < 8) ? (float)(256 + 128 * (c16 >> 2) + 2 * (c16 & 3)) : (float)(512 + 128 * ((c16 - 8) >> 1) + 4 * (c16 & 1));
    L1[2] = (float)(1024 + 128 * (c16 < 9 ? c16 : 0));
    const float L2 = (float)(8 * c16);
    f4 acc[3] = {f4{0.f, 0.f, 0.f, 0.f}, f4{0.f, 0.f, 0.f, 0.f}, f4{0.f, 0.f, 0.f, 0.f}};
    const int n0 = d * 128 + hw * 64 + g * 16;
    v4u pr[6][2];
#pragma unroll
    for (int q = 0; q < 2; q++) {
      pr[0][q] = *(const v4u*)(p.Are + n0 + 8 * q); pr[1][q] = *(const v4u*)(p.Aim + n0 + 8 * q);
      pr[2][q] = *(const v4u*)(p.Bre + n0 + 8 * q); pr[3][q] = *(const v4u*)(p.Bim + n0 + 8 * q);
      pr[4][q] = *(const v4u*)(p.Cre + n0 + 8 * q); pr[5][q] = *(const v4u*)(p.Cim + n0 + 8 * q);
    }
#pragma unroll
    for (int m = 0; m < 16; m++) {
#define PRM(k) __uint_as_float((m & 1) ? (pr[k][m >> 3][(m >> 1) & 3] & 0xFFFF0000u) : (pr[k][m >> 3][(m >> 1) & 3] << 16))
      const float a = dt * PRM(0), b = dt * PRM(1);
      const float br = dt * PRM(2), bi = dt * PRM(3), cr = PRM(4), ci = PRM(5);
#undef PRM
      const float cbr = cr * br - ci * bi, cbi = cr * bi + ci * br;
      const float a2 = a * 1.4426950408889634f, brev = b * 0.15915494309189535f;
      float yr, yi; cexp2(a2, brev, L2, yr, yi);
#pragma unroll
      for (int t = 0; t < 3; t++) {
        float er, ei; cexp2(a2, brev, L1[t], er, ei);
        float xr = cbr * er - cbi * ei, xi = cbr * ei + cbi * er;
        acc[t] = __builtin_amdgcn_mfma_f32_16x16x4f32(xr, yr, acc[t], 0, 0, 0);
        acc[t] = __builtin_amdgcn_mfma_f32_16x16x4f32(-xi, yi, acc[t], 0, 0, 0);
      }
    }
    LAS float* kd = (LAS float*)(lds + S_KD + w * 3072);
#pragma unroll
    for (int t = 0; t < 3; t++)
#pragma unroll
      for (int i = 0; i < 4; i++) kd[(16 * t + 4 * g + i) * 16 + c16] = acc[t][i];
  }
  wgbar();
#pragma unroll
  for (int i = 0; i < 4; i++) {
    const int l = i * 512 + tid;
    const int lr = (int)rbf((float)l);
    int row, j;
    if (lr < 256) { j = (lr >> 3) & 15; row = (lr & 7) + 8 * (lr >> 7); }
    else if (lr < 512) { int x = lr - 256; j = (x & 127) >> 3; row = 16 + 4 * (x >> 7) + ((x & 7) >> 1); }
    else if (lr < 1024) { int x = lr - 512; j = (x & 127) >> 3; row = 24 + 2 * (x >> 7) + ((x & 7) >> 2); }
    else { int x = lr - 1024; j = (x & 127) >> 3; row = 32 + (x >> 7); }
#pragma unroll
    for (int c = 0; c < 4; c++) {
      const LAS float* kd = (const LAS float*)(lds + S_KD + c * 6144);
      float k = kd[row * 16 + j] + kd[768 + row * 16 + j];
      float hi = rbf(k);
      LAS unsigned char* rv = lds + c * S_CH + 4 * S_UC + (LAGMAX - l) * 2;
      *(LAS unsigned short*)(rv) = f2bfu(hi);
      *(LAS unsigned short*)(rv + S_RV) = f2bfu(k - hi);
    }
  }
  wgbar();
  f4 acc[4];
#pragma unroll
  for (int t = 0; t < 4; t++) acc[t] = f4{0.f, 0.f, 0.f, 0.f};
  const int A0 = hw, A1 = 3 - hw, A2 = 4 + hw, A3 = 7 - hw;
  const LAS unsigned char* ap = lds + ch * S_CH + 4 * S_UC + (LAGMAX - 16 * c16 + 224 + 8 * g) * 2;
  const LAS unsigned char* bp = lds + ch * S_CH + (c16 & 3) * S_UC + ((c16 & 12) + 224 + 8 * g + 32) * 2;
#define SSM_STEP(T, A) { const LAS v2u* q = (const LAS v2u*)(bp + 512 * (A) - 64 * n); v2u b0 = q[0], b1 = q[1]; v4u bf = {b0.x, b0.y, b1.x, b1.y}; \
    acc[T] = mfma16(ah, bf, acc[T]); acc[T] = mfma16(al, bf, acc[T]); }
  int n = 0;
  for (; n <= 8 * A0 + 8; n++) { v4u ah = lds16(ap - 64 * n), al = lds16(ap - 64 * n + S_RV); SSM_STEP(0, A0) SSM_STEP(1, A1) SSM_STEP(2, A2) SSM_STEP(3, A3) }
  for (; n <= 8 * A1 + 8; n++) { v4u ah = lds16(ap - 64 * n), al = lds16(ap - 64 * n + S_RV); SSM_STEP(1, A1) SSM_STEP(2, A2) SSM_STEP(3, A3) }
  for (; n <= 8 * A2 + 8; n++) { v4u ah = lds16(ap - 64 * n), al = lds16(ap - 64 * n + S_RV); SSM_STEP(2, A2) SSM_STEP(3, A3) }
  for (; n <= 8 * A3 + 8; n++) { v4u ah = lds16(ap - 64 * n), al = lds16(ap - 64 * n + S_RV); SSM_STEP(3, A3) }
#undef SSM_STEP
  {
    const float dsk = bf2f(p.Dsk[d]);
#pragma unroll
    for (int t = 0; t < 4; t++) {
      const int A = t == 0 ? A0 : t == 1 ? A1 : t == 2 ? A2 : A3;
#pragma unroll
      for (int i = 0; i < 4; i++) {
        const int l = 256 * A + 16 * (4 * g + i) + c16;
        float u = bf2f(*(const LAS unsigned short*)(lds + ch * S_CH + (l + 32) * 2));
        float b = rbf(acc[t][i]) + rbf(u * dsk);
        *(LAS unsigned short*)(lds + S_KD + l * 8 + ch * 2) = f2bfu(b);
      }
    }
  }
  wgbar();
#pragma unroll
  for (int i = 0; i < 4; i++) {
    const int l = i * 512 + tid;
    st8(rs, OFF_YB + (unsigned)(l * 1024 + d0 * 2), *(const LAS v2u*)(lds + S_KD + l * 8));
  }
}

__device__ __forceinline__ void gate_unit(const Params& p, rsrc_t rs, int r0) {
  const int tid = threadIdx.x, lane = tid & 63, w = tid >> 6;
  v4u gw = *(const v4u*)(p.gw + lane * 8);
  for (int i = 0; i < 8; i++) {
    const int row = r0 + w * 8 + i;
    v4u xv = *(const v4u*)(p.x + (size_t)row * 512 + lane * 8);
    float s = 0.f;
#pragma unroll
    for (int k = 0; k < 4; k++) {
      s += rbf(__uint_as_float(xv[k] << 16) * __uint_as_float(gw[k] << 16));
      s += rbf(__uint_as_float(xv[k] & 0xFFFF0000u) * __uint_as_float(gw[k] & 0xFFFF0000u));
    }
#pragma unroll
    for (int o = 32; o > 0; o >>= 1) s += __shfl_xor(s, o);
    s = rbf(s);
    float gg = rbf(1.f / (1.f + expf(-s)));
    if (lane == 0) st4(rs, OFF_G + (unsigned)row * 4u, __float_as_uint(gg));
  }
}

__device__ __forceinline__ void out_unit(const Params& p, LAS unsigned char* lds, rsrc_t rs, int s, int cg, bool& dead) {
  const int tid = threadIdx.x, lane = tid & 63, w = __builtin_amdgcn_readfirstlane(tid >> 6);
  const int c16 = lane & 15, g = lane >> 4;
  const unsigned sb = OFF_STEP + (unsigned)s * STEP_BYTES;
  if (w == 0) wait_flags<32>(rs, OFF_UXF + (unsigned)(s * 32 + (lane & 31)) * 4u, dead);
  wgbar();
  v4u ux[4][4];
#pragma unroll
  for (int ct = 0; ct < 4; ct++)
#pragma unroll
    for (int i = 0; i < 4; i++) ux[ct][i] = ld16(rs, OFF_UX + (unsigned)s * 1048576u + (unsigned)(((4 * cg + ct) * 32 + 4 * w + i) * 1024 + lane * 16));
  const int erow = tid >> 4, ecol = 64 * cg + 4 * (tid & 15);
  const v2u yb = ld8(rs, OFF_YB + (unsigned)((32 * s + erow) * 1024 + ecol * 2));
  const float gg = __uint_as_float(ld4(rs, OFF_G + (unsigned)(32 * s + erow) * 4u));
  if (w == 0) {
    if (s < 62) wait_flags<32>(rs, OFF_PXF + (unsigned)(s * 64 + lane) * 4u, dead);
    else wait_flags<6>(rs, OFF_PXF + (unsigned)(s * 64 + lane) * 4u, dead);
  }
  wgbar();
  const unsigned xb = OFF_PX + (unsigned)s * 81920u;
  v4u pa[2][5];
#pragma unroll
  for (int rt = 0; rt < 2; rt++) {
#pragma unroll
    for (int i = 0; i < 4; i++) pa[rt][i] = ld16(rs, xb + (unsigned)(((4 * w + i) * 2 + rt) * 1024 + lane * 16));
    pa[rt][4] = ld16(rs, xb + 65536u + (unsigned)((w * 2 + rt) * 1024 + lane * 16));
  }
  v4u um[4];
  {
    unsigned o[4];
#pragma unroll
    for (int ct = 0; ct < 4; ct++) o[ct] = sb + ST_UM + (unsigned)((((4 * cg + ct) * 32 + 4 * w + g) * 16 + c16) * 16);
    poll16<4>(rs, o, um, dead);
  }
#pragma unroll
  for (int rt = 0; rt < 2; rt++)
#pragma unroll
    for (int ct = 0; ct < 4; ct++) {
      f4 acc = {0.f, 0.f, 0.f, 0.f};
#pragma unroll
      for (int i = 0; i < 4; i++) acc = mfma16(pa[rt][i], ux[ct][i], acc);
      acc = mfma16(pa[rt][4], um[ct], acc);
      *(LAS f4*)(lds + w * 8192 + (rt * 4 + ct) * 1024 + lane * 16) = acc;
    }
  wgbar();
  {
    f4 t = *(const LAS f4*)(lds + tid * 16);
#pragma unroll
    for (int k = 1; k < 8; k++) t += *(const LAS f4*)(lds + k * 8192 + tid * 16);
    const int rt = w >> 2, ct = w & 3;
    LAS float* at = (LAS float*)(lds + 65536);
#pragma unroll
    for (int i = 0; i < 4; i++) at[(16 * rt + 4 * g + i) * 68 + 16 * ct + c16] = t[i];
  }
  wgbar();
  {
    f4 a = *(const LAS f4*)(lds + 65536 + (erow * 68 + 4 * (tid & 15)) * 4);
    const float og = rbf(1.f - gg);
    float b0 = __uint_as_float(yb.x << 16), b1 = __uint_as_float(yb.x & 0xFFFF0000u);
    float b2 = __uint_as_float(yb.y << 16), b3 = __uint_as_float(yb.y & 0xFFFF0000u);
    float o0 = rbf(gg * rbf(a[0])) + rbf(og * b0), o1 = rbf(gg * rbf(a[1])) + rbf(og * b1);
    float o2 = rbf(gg * rbf(a[2])) + rbf(og * b2), o3 = rbf(gg * rbf(a[3])) + rbf(og * b3);
    *(v2u*)(p.out + (size_t)(32 * s + erow) * 512 + ecol) = v2u{pk2(o0, o1), pk2(o2, o3)};
  }
}

__device__ __forceinline__ void role_spare(const Params& p, LAS unsigned char* lds, rsrc_t rs, int j) {
  const int tid = threadIdx.x, w = __builtin_amdgcn_readfirstlane(tid >> 6);
  bool dead = false;
  const v4u sv = {SENT, SENT, SENT, SENT};
  STAMP(blockIdx.x, 0, 0);
  constexpr unsigned N0 = INIT0_STEPS * STEP_BYTES / 16, N1 = 64u * STEP_BYTES / 16;
  warm_weights(p, j & 7, 4 + (j >> 3));
  for (unsigned i = (unsigned)(j * 512 + tid); i < N0; i += NSPARE * 512) st16(rs, OFF_STEP + i * 16u, sv);
  drain(); wgbar();
  if (tid == 0) st4(rs, OFF_CTR + CTR_INIT0 + (unsigned)j * 4u, 1u);
  STAMP(blockIdx.x, 0, 1);
  for (unsigned i = N0 + (unsigned)(j * 512 + tid); i < N1; i += NSPARE * 512) st16(rs, OFF_STEP + i * 16u, sv);
  drain(); wgbar();
  if (tid == 0) st4(rs, OFF_CTR + CTR_INIT1 + (unsigned)j * 4u, 1u);
  STAMP(blockIdx.x, 0, 2);
  if (j < 128) ssm_unit(p, lds, rs, 4 * j); else gate_unit(p, rs, 64 * (j - 128));
  drain(); wgbar();
  if (tid == 0) st4(rs, OFF_CTR + CTR_SSM + (unsigned)j * 4u, 1u);
  STAMP(blockIdx.x, 0, 3);
  if (w == 0) { wait_ctr(rs, OFF_CTR + CTR_SSM, dead); wait_ctr(rs, OFF_CTR + CTR_INIT1, dead); }
  wgbar();
  STAMP(blockIdx.x, 0, 4);
  for (int u = j; u < 512; u += NSPARE) { out_unit(p, lds, rs, u >> 3, u & 7, dead); STAMP(blockIdx.x, 1 + u / NSPARE, 5); }
}

__global__ void __launch_bounds__(512, 2) fused_kernel(Params p) {
  extern __shared__ __attribute__((aligned(16))) unsigned char smem[];
  LAS unsigned char* lds = (LAS unsigned char*)smem;
  const rsrc_t rs = __builtin_amdgcn_make_buffer_rsrc((void*)p.ws, 0, (int)WS_BYTES, 0x00020000);
  const int b = blockIdx.x;
  if (b < 32) role_qk(p, lds, rs, b);
  else if (b < 64) role_vo(p, lds, rs, b - 32);
  else if (b < 96) role_r(p, lds, rs, b - 64);
  else role_spare(p, lds, rs, b - 96);
}

extern "C" void kernel_launch(void* const* d_in, const int* in_sizes, int n_in, void* d_out, int out_size,
                              void* d_ws, size_t ws_size, hipStream_t stream) {
  Params p;
  p.x = (const unsigned short*)d_in[0]; p.mem0 = (const unsigned short*)d_in[1];
  p.Wq = (const unsigned short*)d_in[2]; p.Wk = (const unsigned short*)d_in[3];
  p.Wv = (const unsigned short*)d_in[4]; p.Wo = (const unsigned short*)d_in[5];
  p.Are = (const unsigned short*)d_in[8]; p.Aim = (const unsigned short*)d_in[9];
  p.Bre = (const unsigned short*)d_in[10]; p.Bim = (const unsigned short*)d_in[11];
  p.Cre = (const unsigned short*)d_in[12]; p.Cim = (const unsigned short*)d_in[13];
  p.logdt = (const unsigned short*)d_in[14]; p.Dsk = (const unsigned short*)d_in[15];
  p.gw = (const unsigned short*)d_in[16];
  p.out = (unsigned short*)d_out; p.ws = (unsigned char*)d_ws;
  (void)hipFuncSetAttribute((const void*)fused_kernel, hipFuncAttributeMaxDynamicSharedMemorySize, LDS_BYTES);
  (void)hipMemsetAsync(d_ws, 0, CTR_BYTES, stream);
  fused_kernel<<<256, 512, LDS_BYTES, stream>>>(p);
}
```

```cpp
#include <hip/hip_runtime.h>
#include <hip/hip_bf16.h>
#include <stdint.h>

typedef unsigned v4u __attribute__((ext_vector_type(4)));
typedef unsigned v2u __attribute__((ext_vector_type(2)));
typedef float f4 __attribute__((ext_vector_type(4)));
typedef float f2 __attribute__((ext_vector_type(2)));
typedef __bf16 bf8 __attribute__((ext_vector_type(8)));
typedef __bf16 bf16x16 __attribute__((ext_vector_type(16)));
typedef __bf16 bf2 __attribute__((ext_vector_type(2)));
typedef short s4 __attribute__((ext_vector_type(4)));
typedef unsigned v8u __attribute__((ext_vector_type(8)));
#define LAS __attribute__((address_space(3)))
typedef __amdgpu_buffer_rsrc_t rsrc_t;

constexpr unsigned SENT = 0xFFFFFFFFu;
constexpr int AUX_SC1 = 16;
constexpr unsigned OFF_CTR = 0;
constexpr unsigned CTR_INIT0 = 0, CTR_INIT1 = 1024, CTR_SSM = 2048;
constexpr unsigned OFF_UXF = 4096;
constexpr unsigned OFF_PXF = 16384;
constexpr unsigned CTR_BYTES = 32768;
constexpr unsigned OFF_STEP = 65536;
constexpr unsigned ST_MEM = 0;
constexpr unsigned ST_P = 65536;
constexpr unsigned ST_UM = 65536 + 163840;
constexpr unsigned STEP_BYTES = 65536 + 163840 + 262144;
constexpr unsigned OFF_UX = OFF_STEP + 64u * STEP_BYTES;
constexpr unsigned OFF_PX = OFF_UX + 64u * 1048576u;
constexpr unsigned OFF_YB = OFF_PX + 64u * 81920u;
constexpr unsigned OFF_G = OFF_YB + 2097152u;
constexpr unsigned WS_BYTES = OFF_G + 8192u;
constexpr int INIT0_STEPS = 8;
constexpr int NSPARE = 160;
constexpr unsigned SPIN_CAP = 400000u;
constexpr int LDS_BYTES = 136 * 1024;

#define PROF_DECL
#define PROF_END
#define STAMP(role, s, k) do {} while (0)
#define STAMPR(k) do {} while (0)
struct Params {
  const unsigned short* x; const unsigned short* mem0; const unsigned short* Wq; const unsigned short* Wk;
  const unsigned short* Wv; const unsigned short* Wo; const unsigned short* Are; const unsigned short* Aim;
  const unsigned short* Bre; const unsigned short* Bim; const unsigned short* Cre; const unsigned short* Cim;
  const unsigned short* logdt; const unsigned short* Dsk; const unsigned short* gw;
  unsigned short* out; unsigned char* ws;
};

__device__ __forceinline__ float bf2f(unsigned short u) { return __uint_as_float((unsigned)u << 16); }
__device__ __forceinline__ unsigned pk2(float lo, float hi) {
  bf2 v; v[0] = (__bf16)lo; v[1] = (__bf16)hi; return __builtin_bit_cast(unsigned, v);
}
__device__ __forceinline__ float rbf(float f) { return (float)(__bf16)f; }
__device__ __forceinline__ unsigned short f2bfu(float f) { return __builtin_bit_cast(unsigned short, (__bf16)f); }
__device__ __forceinline__ unsigned san(unsigned u) { return u < 0xFFFFFFFEu ? u : 0xFFFFFFFEu; }
__device__ __forceinline__ v4u ld16(rsrc_t rs, unsigned off) { return __builtin_amdgcn_raw_buffer_load_b128(rs, off, 0, AUX_SC1); }
__device__ __forceinline__ unsigned ld4(rsrc_t rs, unsigned off) { return __builtin_amdgcn_raw_buffer_load_b32(rs, off, 0, AUX_SC1); }
__device__ __forceinline__ v2u ld8(rsrc_t rs, unsigned off) { return __builtin_amdgcn_raw_buffer_load_b64(rs, off, 0, AUX_SC1); }
__device__ __forceinline__ void st16(rsrc_t rs, unsigned off, v4u v) { __builtin_amdgcn_raw_buffer_store_b128(v, rs, off, 0, AUX_SC1); }
__device__ __forceinline__ void st8(rsrc_t rs, unsigned off, v2u v) { __builtin_amdgcn_raw_buffer_store_b64(v, rs, off, 0, AUX_SC1); }
__device__ __forceinline__ void st4(rsrc_t rs, unsigned off, unsigned v) { __builtin_amdgcn_raw_buffer_store_b32(v, rs, off, 0, AUX_SC1); }
__device__ __forceinline__ void cbar() { asm volatile("" ::: "memory"); }
__device__ __forceinline__ void drain() { asm volatile("s_waitcnt vmcnt(0)" ::: "memory"); }
__device__ __forceinline__ void wgbar() {
  asm volatile("s_waitcnt lgkmcnt(0)" ::: "memory"); __builtin_amdgcn_s_barrier(); asm volatile("" ::: "memory");
}
__device__ __forceinline__ unsigned mx4(v4u v) { return max(max(v.x, v.y), max(v.z, v.w)); }
__device__ __forceinline__ v4u lds16(const LAS unsigned char* p) { return *(const LAS v4u*)p; }
__device__ __forceinline__ v2u ldstr(const LAS unsigned char* p) {
  return __builtin_bit_cast(v2u, __builtin_amdgcn_ds_read_tr16_b64_v4i16((LAS s4*)p));
}
__device__ __forceinline__ bf8 asbf8(v4u v) { return __builtin_bit_cast(bf8, v); }
__device__ __forceinline__ f4 mfma16(v4u a, v4u b, f4 c) { return __builtin_amdgcn_mfma_f32_16x16x32_bf16(asbf8(a), asbf8(b), c, 0, 0, 0); }
__device__ __forceinline__ f4 smf(v4u a, v4u b0, v4u b1, f4 c, int idx) {
  v8u b = {b0.x, b0.y, b0.z, b0.w, b1.x, b1.y, b1.z, b1.w};
  return __builtin_amdgcn_smfmac_f32_16x16x64_bf16(asbf8(a), __builtin_bit_cast(bf16x16, b), c, idx, 0, 0);
}
__device__ __forceinline__ float x32(float v) {
  unsigned u = __float_as_uint(v);
  auto r = __builtin_amdgcn_permlane32_swap(u, u, false, false);
  return __uint_as_float((threadIdx.x & 32) ? r[0] : r[1]);
}
__device__ __forceinline__ float add32(float v) { unsigned u = __float_as_uint(v); auto r = __builtin_amdgcn_permlane32_swap(u, u, false, false); return __uint_as_float(r[0]) + __uint_as_float(r[1]); }
__device__ __forceinline__ float max32(float v) { unsigned u = __float_as_uint(v); auto r = __builtin_amdgcn_permlane32_swap(u, u, false, false); return fmaxf(__uint_as_float(r[0]), __uint_as_float(r[1])); }
__device__ __forceinline__ float add16(float v) { unsigned u = __float_as_uint(v); auto r = __builtin_amdgcn_permlane16_swap(u, u, false, false); return __uint_as_float(r[0]) + __uint_as_float(r[1]); }
__device__ __forceinline__ float max16(float v) { unsigned u = __float_as_uint(v); auto r = __builtin_amdgcn_permlane16_swap(u, u, false, false); return fmaxf(__uint_as_float(r[0]), __uint_as_float(r[1])); }
__device__ __forceinline__ float addror8(float v) {
  int r = __builtin_amdgcn_update_dpp(0, __float_as_int(v), 0x128, 0xF, 0xF, false);
  return v + __int_as_float(r);
}
__device__ __forceinline__ float fold2(float a, float b) {
  auto r = __builtin_amdgcn_permlane32_swap(__float_as_uint(a), __float_as_uint(b), false, false);
  return __uint_as_float(r[0]) + __uint_as_float(r[1]);
}

__device__ __forceinline__ void wait_ctr(rsrc_t rs, unsigned off, bool& dead) {
  unsigned spins = dead ? SPIN_CAP : 0;
  const unsigned lane = threadIdx.x & 63;
  while (true) {
    cbar();
    v4u v = ld16(rs, off + (lane < NSPARE / 4 ? lane : 0u) * 16u);
    if (__all(min(min(v.x, v.y), min(v.z, v.w)) != 0u)) { cbar(); return; }
    if (++spins > SPIN_CAP) { dead = true; return; }
    __builtin_amdgcn_s_sleep(8);
  }
}
template <int SL>
__device__ __forceinline__ void wait_flags(rsrc_t rs, unsigned off, bool& dead) {
  unsigned spins = dead ? SPIN_CAP : 0;
  while (true) {
    cbar();
    unsigned v = ld4(rs, off);
    if (__all(v != 0u)) { cbar(); return; }
    if (++spins > SPIN_CAP) { dead = true; return; }
    __builtin_amdgcn_s_sleep(SL);
  }
}
template <int C>
__device__ __forceinline__ void poll16(rsrc_t rs, const unsigned (&off)[C], v4u (&v)[C], bool& dead) {
  unsigned spins = dead ? SPIN_CAP : 0;
  while (true) {
    cbar();
#pragma unroll
    for (int c = 0; c < C; c++) v[c] = ld16(rs, off[c]);
    unsigned m = 0;
#pragma unroll
    for (int c = 0; c < C; c++) m = max(m, mx4(v[c]));
    if (__all(m != SENT)) return;
    if (++spins > SPIN_CAP) { dead = true; return; }
  }
}

template <int NCT>
__device__ __forceinline__ void wload_issue(const unsigned short* src, int rowstride, int lane, v4u (&t)[NCT * 2]) {
  constexpr int LPR = NCT * 2;
  constexpr int RPI = 64 / LPR;
#pragma unroll
  for (int i = 0; i < NCT * 2; i++) {
    int row = i * RPI + lane / LPR, pc = lane % LPR;
    t[i] = *(const v4u*)(src + (size_t)row * rowstride + pc * 8);
  }
}
template <int NCT>
__device__ __forceinline__ void wload_finish(LAS unsigned char* slab, int lane, const v4u (&t)[NCT * 2], v4u (&f)[NCT][2]) {
  constexpr int LPR = NCT * 2, RPI = 64 / LPR, RB = NCT * 32;
#pragma unroll
  for (int i = 0; i < NCT * 2; i++) {
    int row = i * RPI + lane / LPR, pc = lane % LPR;
    *(LAS v4u*)(slab + row * RB + pc * 16) = t[i];
  }
  asm volatile("s_waitcnt lgkmcnt(0)" ::: "memory");
  int gb = lane >> 4, q = (lane >> 2) & 3, p = lane & 3;
#pragma unroll
  for (int ct = 0; ct < NCT; ct++)
#pragma unroll
    for (int hf = 0; hf < 2; hf++) {
      v2u a = ldstr(slab + (32 * hf + 8 * gb + q) * RB + ct * 32 + p * 8);
      v2u b = ldstr(slab + (32 * hf + 8 * gb + 4 + q) * RB + ct * 32 + p * 8);
      f[ct][hf] = v4u{a.x, a.y, b.x, b.y};
    }
  asm volatile("s_waitcnt lgkmcnt(0)" ::: "memory");
}

constexpr int WROW = 272;
__device__ __forceinline__ v4u wrow_issue(const unsigned short* src, int row0, int tid) {
  return *(const v4u*)(src + (size_t)(row0 + (tid >> 4)) * 4096 + (tid & 15) * 8);
}
__device__ __forceinline__ void wrow_store(LAS unsigned char* img, int row0, int tid, v4u v) {
  *(LAS v4u*)(img + (row0 + (tid >> 4)) * WROW + (tid & 15) * 16) = v;
}
__device__ __forceinline__ v4u wrow_frag(const LAS unsigned char* img, int row0, int colbyte, int lane) {
  const int gb = lane >> 4, q = (lane >> 2) & 3, p = lane & 3;
  v2u a = ldstr(img + (row0 + 8 * gb + q) * WROW + colbyte + p * 8);
  v2u b = ldstr(img + (row0 + 8 * gb + 4 + q) * WROW + colbyte + p * 8);
  return v4u{a.x, a.y, b.x, b.y};
}
constexpr int L_XS = 0;
constexpr int L_MEM = 65536;
constexpr int L_QKT = 73728;
constexpr int L_QXT = 81920;
constexpr int L_KXT = 90112;
constexpr int L_KXP = 98304;
constexpr int L_SLAB = 0;

__device__ __forceinline__ void x_dma(const Params& p, LAS unsigned char* lds, int s, int w, int lane) {
#pragma unroll
  for (int i = 0; i < 4; i++) {
    const int r = 4 * w + i;
    const unsigned short* src = p.x + (size_t)(s * 32 + r) * 512 + (lane ^ (r & 15)) * 8;
    const unsigned dst = __builtin_amdgcn_readfirstlane((unsigned)(uintptr_t)(lds + L_XS + (s & 1) * 32768 + r * 1024));
    unsigned keep;
    asm volatile("s_mov_b32 %0, m0\n\ts_mov_b32 m0, %2\n\ts_nop 0\n\tglobal_load_lds_dwordx4 %1, off\n\ts_mov_b32 m0, %0"
                 : "=&s"(keep) : "v"(src), "s"(dst) : "memory");
  }
}
__device__ __forceinline__ v4u x_frag(const LAS unsigned char* lds, int s, int rt, int ks, int c16, int g) {
  return lds16(lds + ((s & 1) * 32768 + c16 * 1024 + ((4 * (ks & 3) + g) ^ c16) * 16) + (L_XS + rt * 16384 + (ks >> 2) * 256));
}
__device__ __forceinline__ int t64(int e, int gam) { return e * 64 + ((gam ^ (e >> 1)) & 7) * 8; }
__device__ __forceinline__ v4u t64_frag(const LAS unsigned char* img, int ks, int half, int g, int tq, int tp) {
  v2u a = ldstr(img + ks * 2048 + t64(8 * g + tq, 4 * half + tp));
  v2u b = ldstr(img + ks * 2048 + t64(8 * g + 4 + tq, 4 * half + tp));
  return v4u{a.x, a.y, b.x, b.y};
}
__device__ __forceinline__ v4u t16_frag(const LAS unsigned char* img, int ks, int g, int tq, int tp) {
  const int o = (8 * g + tq) * 16 + ((tp ^ g) & 1) * 8;
  v2u a = ldstr(img + ks * 512 + o);
  v2u b = ldstr(img + ks * 512 + 64 + o);
  return v4u{a.x, a.y, b.x, b.y};
}
__device__ __forceinline__ v4u mem0_chunk(const Params& p, int w, int lane) {
  int r = lane & 15, ga = lane >> 4;
  const unsigned* m = (const unsigned*)(p.mem0 + (r & 7) * 512 + 64 * w + 16 * ga + 2 * (r >> 3));
  return v4u{m[0], m[2], m[4], m[6]};
}

constexpr float SCL2 = 0.08838834764831845f * 1.4426950408889634f;

template <int CLS>
__device__ __forceinline__ void qk_loop(const Params& p, LAS unsigned char* lds, rsrc_t rs, int h, const v4u (&Wf)[8][2][2], bool dead) {
  const int tid = threadIdx.x, lane = tid & 63, w = __builtin_amdgcn_readfirstlane(tid >> 6);
  const int c16 = lane & 15, g = lane >> 4, tq = (lane >> 2) & 3, tp = lane & 3;
  const int idx = (c16 < 8) ? 0x4444 : 0xEEEE;
  const unsigned cp = (unsigned)(blockIdx.x & 7);
  PROF_DECL
  for (int s = 0; s < 64; s++) {
    STAMP(blockIdx.x, s, 0);
    {
      f4 acc[2][2];
#pragma unroll
      for (int rt = 0; rt < 2; rt++)
#pragma unroll
        for (int ct = 0; ct < 2; ct++) acc[rt][ct] = f4{0.f, 0.f, 0.f, 0.f};
      if (s < 63) x_dma(p, lds, s + 1, w, lane);
      v4u a[2][2][2];
#pragma unroll
      for (int k = 0; k < 2; k++)
#pragma unroll
        for (int rt = 0; rt < 2; rt++) a[0][k][rt] = x_frag(lds, s, rt, k, c16, g);
#pragma unroll
      for (int kk = 0; kk < 8; kk++) {
        if (kk < 7) {
#pragma unroll
          for (int k = 0; k < 2; k++)
#pragma unroll
            for (int rt = 0; rt < 2; rt++) a[(kk + 1) & 1][k][rt] = x_frag(lds, s, rt, 2 * kk + 2 + k, c16, g);
        }
        __builtin_amdgcn_sched_barrier(0);
#pragma unroll
        for (int k = 0; k < 2; k++)
#pragma unroll
          for (int rt = 0; rt < 2; rt++)
#pragma unroll
            for (int ct = 0; ct < 2; ct++) acc[rt][ct] = mfma16(a[kk & 1][k][rt], Wf[kk][ct][k], acc[rt][ct]);
        __builtin_amdgcn_sched_barrier(0);
      }
#pragma unroll
      for (int rt = 0; rt < 2; rt++)
#pragma unroll
        for (int ct = 0; ct < 2; ct++) {
          int e = 32 * (w & 3) + 16 * ct + c16;
          unsigned p0 = pk2(acc[rt][ct][0], acc[rt][ct][1]), p1 = pk2(acc[rt][ct][2], acc[rt][ct][3]);
          *(LAS v2u*)(lds + (CLS < 2 ? L_QXT : L_KXT) + t64(e, 4 * rt + g)) = v2u{p0, p1};
          if (CLS == 2) {
            const int gam = (g & 1) * 4 + (g >> 1) * 2;
            *(LAS unsigned*)(lds + L_KXP + t64(e, gam) + rt * 4) = p0;
            *(LAS unsigned*)(lds + L_KXP + t64(e, gam + 1) + rt * 4) = p1;
          }
        }
    }
    STAMP(blockIdx.x, s, 6);
    wgbar();
    v4u fr[8];
    if (CLS == 0) {
#pragma unroll
      for (int tau = 0; tau < 2; tau++)
#pragma unroll
        for (int ks = 0; ks < 4; ks++) fr[tau * 4 + ks] = t64_frag(lds + L_KXP, ks, tau, g, tq, tp);
    } else if (CLS == 1) {
      const int rt = w - 2;
      f4 sxx[2] = {f4{0.f, 0.f, 0.f, 0.f}, f4{0.f, 0.f, 0.f, 0.f}};
      v4u kf[2][4];
#pragma unroll
      for (int ks = 0; ks < 4; ks++) {
        fr[ks] = t64_frag(lds + L_QXT, ks, rt, g, tq, tp);
        kf[0][ks] = t64_frag(lds + L_KXT, ks, 0, g, tq, tp);
        kf[1][ks] = t64_frag(lds + L_KXT, ks, 1, g, tq, tp);
      }
#pragma unroll
      for (int ks = 0; ks < 4; ks++)
#pragma unroll
        for (int tau = 0; tau < 2; tau++) sxx[tau] = mfma16(kf[tau][ks], fr[ks], sxx[tau]);
      fr[4] = __builtin_bit_cast(v4u, sxx[0]); fr[5] = __builtin_bit_cast(v4u, sxx[1]);
    } else if (w == 7 && s == INIT0_STEPS - 1) {
      wait_ctr(rs, OFF_CTR + CTR_INIT1, dead);
    }
    const unsigned sb = OFF_STEP + (unsigned)s * STEP_BYTES;
    STAMP(blockIdx.x, s, 1);
    {
      v4u m[1];
      if (s == 0) m[0] = mem0_chunk(p, w, lane);
      else { const unsigned o[1] = {sb + ST_MEM + cp * 8192u + (unsigned)tid * 16u}; poll16<1>(rs, o, m, dead); }
      STAMP(blockIdx.x, s, 2);
      *(LAS v4u*)(lds + L_MEM + tid * 16) = m[0];
      drain();
    }
    wgbar();
    STAMP(blockIdx.x, s, 3);
    {
      f4 acc[2] = {f4{0.f, 0.f, 0.f, 0.f}, f4{0.f, 0.f, 0.f, 0.f}};
      v4u a[8];
#pragma unroll
      for (int t = 0; t < 8; t++) a[t] = lds16(lds + L_MEM + t * 1024 + lane * 16);
      __builtin_amdgcn_sched_barrier(0);
#pragma unroll
      for (int t = 0; t < 8; t++)
#pragma unroll
        for (int ct = 0; ct < 2; ct++) acc[ct] = smf(a[t], Wf[t][ct][0], Wf[t][ct][1], acc[ct], idx);
      float f[4];
#pragma unroll
      for (int i = 0; i < 4; i++) f[i] = fold2(acc[0][i], acc[1][i]);
      *(LAS v2u*)(lds + L_QKT + (32 * w + 16 * (g >> 1) + c16) * 16 + ((g ^ (c16 >> 3)) & 1) * 8) = v2u{pk2(f[0], f[1]), pk2(f[2], f[3])};
    }
    wgbar();
    STAMP(blockIdx.x, s, 4);
    if (CLS == 0) {
      if (s < 63) {
        f4 sx0 = {0.f, 0.f, 0.f, 0.f}, sx1 = sx0, sm = sx0;
        v4u qf[4], kf[4];
#pragma unroll
        for (int ks = 0; ks < 4; ks++) { qf[ks] = t16_frag(lds + L_QKT, ks, g, tq, tp); kf[ks] = t16_frag(lds + L_QKT + 2048, ks, g, tq, tp); }
        __builtin_amdgcn_sched_barrier(0);
#pragma unroll
        for (int ks = 0; ks < 4; ks++) {
          sx0 = mfma16(fr[ks], qf[ks], sx0);
          sx1 = mfma16(fr[4 + ks], qf[ks], sx1);
          sm = mfma16(kf[ks], qf[ks], sm);
        }
        float v[12];
#pragma unroll
        for (int i = 0; i < 4; i++) { v[i] = sx0[i]; v[4 + i] = sx1[i]; v[8 + i] = sm[i]; }
        float mx = fmaxf(fmaxf(fmaxf(v[0], v[1]), fmaxf(fmaxf(v[2], v[3]), fmaxf(v[4], v[5]))), fmaxf(fmaxf(fmaxf(v[6], v[7]), v[8]), fmaxf(fmaxf(v[9], v[10]), v[11])));
        mx = max32(max16(mx));
        const float nb = -mx * SCL2;
#pragma unroll
        for (int i = 0; i < 12; i++) v[i] = __builtin_amdgcn_exp2f(__builtin_fmaf(v[i], SCL2, nb));
        float sum = ((v[0] + v[1]) + (v[2] + v[3])) + ((v[4] + v[5]) + (v[6] + v[7])) + 0.5f * ((v[8] + v[9]) + (v[10] + v[11]));
        sum = add32(add16(sum));
        float inv = __builtin_amdgcn_rcpf(sum);
        v4u px = {san(pk2(v[0] * inv, v[1] * inv)), san(pk2(v[2] * inv, v[3] * inv)), san(pk2(v[4] * inv, v[5] * inv)), san(pk2(v[6] * inv, v[7] * inv))};
        v2u pm = {san(pk2(v[8] * inv, v[9] * inv)), san(pk2(v[10] * inv, v[11] * inv))};
        const unsigned pb = sb + ST_P + (unsigned)(4 * w + (c16 >> 3)) * 20480u;
        const unsigned ox = (unsigned)(h * 512 + (g * 8 + (c16 & 7)) * 16);
        st16(rs, pb + ox, px);
        st16(rs, pb + 40960u + ox, px);
        st8(rs, pb + (unsigned)(g >> 1) * 40960u + 16384u + (unsigned)((((h >> 1) * 16) + (g & 1) * 8 + (c16 & 7)) * 16 + (h & 1) * 8), pm);
      }
    } else if (CLS == 1) {
      const int rt = w - 2;
      if (lane == 0 && s > 0) st4(rs, OFF_PXF + (unsigned)((s - 1) * 64 + h * 2 + rt) * 4u, 1u);
      f4 sm = {0.f, 0.f, 0.f, 0.f};
      v4u kf[4];
#pragma unroll
      for (int ks = 0; ks < 4; ks++) kf[ks] = t16_frag(lds + L_QKT + 2048, ks, g, tq, tp);
#pragma unroll
      for (int ks = 0; ks < 4; ks++) sm = mfma16(kf[ks], fr[ks], sm);
      float v[12];
      const int R = 16 * rt + c16;
#pragma unroll
      for (int i = 0; i < 4; i++) {
        v[i] = (4 * g + i <= R) ? __uint_as_float(fr[4][i]) : -3.0e38f;
        v[4 + i] = (16 + 4 * g + i <= R) ? __uint_as_float(fr[5][i]) : -3.0e38f;
        v[8 + i] = sm[i];
      }
      float mx = v[0];
#pragma unroll
      for (int i = 1; i < 12; i++) mx = fmaxf(mx, v[i]);
      mx = max32(max16(mx));
      float sum = 0.f, summ = 0.f;
#pragma unroll
      for (int i = 0; i < 12; i++) {
        float e = __builtin_amdgcn_exp2f((v[i] - mx) * SCL2);
        if (i < 4) e = (4 * g + i <= R) ? e : 0.f; else if (i < 8) e = (12 + 4 * g + i <= R) ? e : 0.f;
        v[i] = e; if (i < 8) sum += e; else summ += e;
      }
      sum += 0.5f * summ;
      sum = add32(add16(sum));
      float inv = __builtin_amdgcn_rcpf(sum);
      v4u px = {pk2(v[0] * inv, v[1] * inv), pk2(v[2] * inv, v[3] * inv), pk2(v[4] * inv, v[5] * inv), pk2(v[6] * inv, v[7] * inv)};
      unsigned pm = (g < 2) ? pk2(v[8] * inv, v[9] * inv) : pk2(v[10] * inv, v[11] * inv);
      const unsigned xb = OFF_PX + (unsigned)s * 81920u;
      st16(rs, xb + (unsigned)((h * 2 + rt) * 1024 + lane * 16), px);
      st4(rs, xb + 65536u + (unsigned)((((h >> 2) * 2 + rt) * 64 + (h & 3) * 16 + c16) * 16 + g * 4), pm);
      if (s == 63) { drain(); if (lane == 0) st4(rs, OFF_PXF + (unsigned)(s * 64 + h * 2 + rt) * 4u, 1u); }
    }
    STAMP(blockIdx.x, s, 5);
    __builtin_amdgcn_s_barrier();
    if (s == 0) STAMPR(2);
  }
  PROF_END; STAMPR(3);
}

__device__ __forceinline__ void role_qk(const Params& p, LAS unsigned char* lds, rsrc_t rs, int h) {
  const int tid = threadIdx.x, lane = tid & 63, w = __builtin_amdgcn_readfirstlane(tid >> 6);
  bool dead = false;
  STAMPR(0);
  v4u Wf[8][2][2];
  {
    v4u t[2][2][8];
#pragma unroll
    for (int r = 0; r < 2; r++)
#pragma unroll
      for (int i = 0; i < 8; i++) {
        t[r][0][i] = wrow_issue(p.Wq + h * 128, 256 * r + 32 * i, tid);
        t[r][1][i] = wrow_issue(p.Wk + h * 128, 256 * r + 32 * i, tid);
      }
#pragma unroll
    for (int r = 0; r < 2; r++) {
#pragma unroll
      for (int i = 0; i < 8; i++) { wrow_store(lds, 32 * i, tid, t[r][0][i]); wrow_store(lds + 256 * WROW, 32 * i, tid, t[r][1][i]); }
      wgbar();
#pragma unroll
      for (int k = 0; k < 4; k++)
#pragma unroll
        for (int ct = 0; ct < 2; ct++)
#pragma unroll
          for (int hf = 0; hf < 2; hf++)
            Wf[4 * r + k][ct][hf] = wrow_frag(lds + (w >> 2) * 256 * WROW, 64 * k + 32 * hf, (w & 3) * 64 + ct * 32, lane);
      wgbar();
    }
  }
  x_dma(p, lds, 0, w, lane);
  if (w == 7) wait_ctr(rs, OFF_CTR + CTR_INIT0, dead);
  drain(); wgbar();

  STAMPR(1);
  if (w < 2) qk_loop<0>(p, lds, rs, h, Wf, dead);
  else if (w < 4) qk_loop<1>(p, lds, rs, h, Wf, dead);
  else qk_loop<2>(p, lds, rs, h, Wf, dead);
}

__device__ __forceinline__ void role_vo(const Params& p, LAS unsigned char* lds, rsrc_t rs, int h) {
  const int tid = threadIdx.x, lane = tid & 63, w = __builtin_amdgcn_readfirstlane(tid >> 6);
  const int c16 = lane & 15, g = lane >> 4, tq = (lane >> 2) & 3, tp = lane & 3;
  const int idx = (c16 < 8) ? 0x4444 : 0xEEEE;
  const unsigned cp = (unsigned)(blockIdx.x & 7);
  bool dead = false;
  PROF_DECL STAMPR(0);
  v4u Vf[8][1][2], Of[2][4][2];
  {
    const unsigned short* so = p.Wo + (size_t)h * 65536 + 64 * w;
    v4u t[16], t2[2][8];
#pragma unroll
    for (int i = 0; i < 16; i++) t[i] = wrow_issue(p.Wv + h * 128, 32 * i, tid);
#pragma unroll
    for (int k = 0; k < 2; k++) wload_issue<4>(so + (size_t)k * 64 * 512, 512, lane, t2[k]);
#pragma unroll
    for (int i = 0; i < 16; i++) wrow_store(lds, 32 * i, tid, t[i]);
    wgbar();
#pragma unroll
    for (int k = 0; k < 8; k++)
#pragma unroll
      for (int hf = 0; hf < 2; hf++) Vf[k][0][hf] = wrow_frag(lds, 64 * k + 32 * hf, w * 32, lane);
    wgbar();
#pragma unroll
    for (int k = 0; k < 2; k++) wload_finish<4>(lds + L_SLAB + w * 8192, lane, t2[k], Of[k]);
    wgbar();
  }
  x_dma(p, lds, 0, w, lane);
  drain(); wgbar();
  STAMPR(1);
  for (int s = -1; s < 64; s++) {
    const int sx = s + 1;
    if (sx < 64) {
      STAMP(blockIdx.x, s, 0);
      {
        f4 acc[2] = {f4{0.f, 0.f, 0.f, 0.f}, f4{0.f, 0.f, 0.f, 0.f}};
        if (sx < 63) x_dma(p, lds, sx + 1, w, lane);
        v4u a[3][2][2];
#pragma unroll
        for (int q = 0; q < 2; q++)
#pragma unroll
          for (int k = 0; k < 2; k++)
#pragma unroll
            for (int rt = 0; rt < 2; rt++) a[q][k][rt] = x_frag(lds, sx, rt, 2 * q + k, c16, g);
#pragma unroll
        for (int kk = 0; kk < 8; kk++) {
          if (kk < 6) {
#pragma unroll
            for (int k = 0; k < 2; k++)
#pragma unroll
              for (int rt = 0; rt < 2; rt++) a[(kk + 2) % 3][k][rt] = x_frag(lds, sx, rt, 2 * kk + 4 + k, c16, g);
          }
          __builtin_amdgcn_sched_barrier(0);
#pragma unroll
          for (int k = 0; k < 2; k++)
#pragma unroll
            for (int rt = 0; rt < 2; rt++) acc[rt] = mfma16(a[kk % 3][k][rt], Vf[kk][0][k], acc[rt]);
          __builtin_amdgcn_sched_barrier(0);
        }
#pragma unroll
        for (int rt = 0; rt < 2; rt++)
          *(LAS v2u*)(lds + L_QXT + t64(16 * w + c16, 4 * rt + g)) = v2u{pk2(acc[rt][0], acc[rt][1]), pk2(acc[rt][2], acc[rt][3])};
      }
      wgbar();
      {
        f4 acc[2][4];
#pragma unroll
        for (int rt = 0; rt < 2; rt++)
#pragma unroll
          for (int ct = 0; ct < 4; ct++) acc[rt][ct] = f4{0.f, 0.f, 0.f, 0.f};
        v4u af[4][2];
#pragma unroll
        for (int ks = 0; ks < 4; ks++)
#pragma unroll
          for (int rt = 0; rt < 2; rt++) af[ks][rt] = t64_frag(lds + L_QXT, ks, rt, g, tq, tp);
        __builtin_amdgcn_sched_barrier(0);
#pragma unroll
        for (int ks = 0; ks < 4; ks++)
#pragma unroll
          for (int rt = 0; rt < 2; rt++)
#pragma unroll
            for (int ct = 0; ct < 4; ct++) acc[rt][ct] = mfma16(af[ks][rt], Of[ks >> 1][ct][ks & 1], acc[rt][ct]);
#pragma unroll
        for (int ct = 0; ct < 4; ct++) {
          v4u u = {pk2(acc[0][ct][0], acc[0][ct][1]), pk2(acc[0][ct][2], acc[0][ct][3]), pk2(acc[1][ct][0], acc[1][ct][1]), pk2(acc[1][ct][2], acc[1][ct][3])};
          st16(rs, OFF_UX + (unsigned)sx * 1048576u + (unsigned)(((4 * w + ct) * 32 + h) * 1024 + lane * 16), u);
        }
      }
    }
    if (s < 0) {
      if (w == 7) wait_ctr(rs, OFF_CTR + CTR_INIT0, dead);
      drain(); wgbar();
      if (tid == 0) st4(rs, OFF_UXF + (unsigned)h * 4u, 1u);
      continue;
    }
    if (w == 7 && s == INIT0_STEPS - 1) wait_ctr(rs, OFF_CTR + CTR_INIT1, dead);
    STAMP(blockIdx.x, s, 1);
    const unsigned sb = OFF_STEP + (unsigned)s * STEP_BYTES;
    {
      v4u m[1];
      if (s == 0) m[0] = mem0_chunk(p, w, lane);
      else { const unsigned o[1] = {sb + ST_MEM + cp * 8192u + (unsigned)tid * 16u}; poll16<1>(rs, o, m, dead); }
      STAMP(blockIdx.x, s, 2);
      *(LAS v4u*)(lds + L_MEM + tid * 16) = m[0];
      drain();
    }
    wgbar();
    if (tid == 64 && sx < 64) st4(rs, OFF_UXF + (unsigned)(sx * 32 + h) * 4u, 1u);
    {
      f4 a0 = {0.f, 0.f, 0.f, 0.f}, a1 = a0;
      v4u a[8];
#pragma unroll
      for (int t = 0; t < 8; t++) a[t] = lds16(lds + L_MEM + t * 1024 + lane * 16);
      __builtin_amdgcn_sched_barrier(0);
#pragma unroll
      for (int t = 0; t < 8; t += 2) {
        a0 = smf(a[t], Vf[t][0][0], Vf[t][0][1], a0, idx);
        a1 = smf(a[t + 1], Vf[t + 1][0][0], Vf[t + 1][0][1], a1, idx);
      }
      a0 += a1;
      float f0 = fold2(a0[0], a0[1]), f1 = fold2(a0[2], a0[3]);
      const int gp = g & 1, up = g >> 1;
      const int hf = (c16 >> 1) & 1, j = 2 * (c16 >> 2) + (c16 & 1);
      LAS unsigned char* b = lds + L_QKT + (w * 16 + hf * 8 + 2 * gp + up) * 16 + j * 2;
      *(LAS unsigned short*)(b) = f2bfu(f0);
      *(LAS unsigned short*)(b + 64) = f2bfu(f1);
    }
    wgbar();
    {
      v4u a0 = lds16(lds + L_QKT + lane * 16), a1 = lds16(lds + L_QKT + 1024 + lane * 16);
      f4 acc[4];
#pragma unroll
      for (int ct = 0; ct < 4; ct++) {
        acc[ct] = smf(a0, Of[0][ct][0], Of[0][ct][1], f4{0.f, 0.f, 0.f, 0.f}, idx);
        acc[ct] = smf(a1, Of[1][ct][0], Of[1][ct][1], acc[ct], idx);
      }
#pragma unroll
      for (int ct = 0; ct < 4; ct += 2) {
        float f[4];
#pragma unroll
        for (int i = 0; i < 4; i++) f[i] = fold2(acc[ct][i], acc[ct + 1][i]);
        v2u u = {san(pk2(f[0], f[1])), san(pk2(f[2], f[3]))};
        st8(rs, sb + ST_UM + (unsigned)((((4 * w + ct + (g >> 1)) * 32 + h) * 16 + c16) * 16 + (g & 1) * 8), u);
      }
    }
    STAMP(blockIdx.x, s, 5);
    if (s == 0) STAMPR(2);
  }
  PROF_END; STAMPR(3);
}

__device__ __forceinline__ void warm_weights(const Params& p, int cls, int q) {
  unsigned acc = 0;
  for (int i = q * 512 + (int)threadIdx.x; i < 16384; i += 24 * 512) {
    const int line = i & 1, row = (i >> 1) & 511, m = (i >> 10) & 3, hh = cls + 8 * (i >> 12);
    const unsigned short* a = m == 3 ? p.Wo + (size_t)hh * 65536 + (row * 2 + line) * 64
                                     : (m == 0 ? p.Wq : m == 1 ? p.Wk : p.Wv) + (size_t)row * 4096 + hh * 128 + line * 64;
    acc += *(const unsigned*)a;
  }
  asm volatile("" :: "v"(acc));
}
__device__ __forceinline__ void role_r(const Params& p, LAS unsigned char* lds, rsrc_t rs, int c) {
  const int tid = threadIdx.x, lane = tid & 63, w = __builtin_amdgcn_readfirstlane(tid >> 6);
  const int c16 = lane & 15, g = lane >> 4;
  const int idx = (c16 < 8) ? 0x4444 : 0xEEEE;
  const unsigned cp = (unsigned)(blockIdx.x & 7);
  bool dead = false;
  PROF_DECL STAMPR(0);
  warm_weights(p, c & 7, c >> 3);
  if (w == 7) wait_ctr(rs, OFF_CTR + CTR_INIT0, dead);
  wgbar();
  for (int s = 0; s < 63; s++) {
    const unsigned sb = OFF_STEP + (unsigned)s * STEP_BYTES;
    if (s == INIT0_STEPS - 2) wait_ctr(rs, OFF_CTR + CTR_INIT1, dead);
    STAMP(blockIdx.x, s, 0);
    wait_flags<4>(rs, OFF_UXF + (unsigned)(s * 32 + 4 * w + (lane & 3)) * 4u, dead);
    STAMP(blockIdx.x, s, 1);
    v4u ux[4];
#pragma unroll
    for (int i = 0; i < 4; i++) ux[i] = ld16(rs, OFF_UX + (unsigned)s * 1048576u + (unsigned)((c * 32 + 4 * w + i) * 1024 + lane * 16));
    f4 acc = {0.f, 0.f, 0.f, 0.f};
    const unsigned pb = sb + ST_P + cp * 20480u;
    if (w < 4) {
      v4u um[2];
      { const unsigned o[2] = {sb + ST_UM + (unsigned)(((c * 32 + 8 * w + g) * 16 + c16) * 16), sb + ST_UM + (unsigned)(((c * 32 + 8 * w + 4 + g) * 16 + c16) * 16)};
        poll16<2>(rs, o, um, dead); }
      STAMP(blockIdx.x, s, 2);
      v4u pa[3];
      { const unsigned o[3] = {pb + (unsigned)(2 * w * 1024 + lane * 16), pb + (unsigned)((2 * w + 1) * 1024 + lane * 16), pb + 16384u + (unsigned)(w * 1024 + lane * 16)};
        poll16<3>(rs, o, pa, dead); }
      acc = smf(pa[0], ux[0], ux[1], acc, idx);
      acc = smf(pa[1], ux[2], ux[3], acc, idx);
      acc = smf(pa[2], um[0], um[1], acc, idx);
    } else {
      v4u pa[2];
      { const unsigned o[2] = {pb + (unsigned)(2 * w * 1024 + lane * 16), pb + (unsigned)((2 * w + 1) * 1024 + lane * 16)};
        poll16<2>(rs, o, pa, dead); }
      acc = smf(pa[0], ux[0], ux[1], acc, idx);
      acc = smf(pa[1], ux[2], ux[3], acc, idx);
    }
    STAMP(blockIdx.x, s, 3);
    {
      float f0 = fold2(acc[0], acc[1]), f1 = fold2(acc[2], acc[3]);
      LAS float* red = (LAS float*)(lds + (s & 1) * 4096) + w * 128 + (4 * (g & 1) + (g >> 1)) * 16 + c16;
      red[0] = f0; red[32] = f1;
    }
    wgbar();
    {
      const int r = lane >> 2, dq = lane & 3;
      const LAS f2* red = (const LAS f2*)((LAS float*)(lds + (s & 1) * 4096) + (r & 7) * 16 + 4 * dq + 2 * (r >> 3));
      f2 a = red[0], b = red[64], c2 = red[128], d = red[192], e = red[256], f = red[320], gg = red[384], hh = red[448];
      f2 t = ((a + b) + (c2 + d)) + ((e + f) + (gg + hh));
      st4(rs, sb + STEP_BYTES + ST_MEM + (unsigned)(w * 8192 + c * 256 + lane * 4), san(pk2(t[0], t[1])));
    }
    STAMP(blockIdx.x, s, 5);
    if (s == 0) STAMPR(2);
  }
  PROF_END; STAMPR(3);
}

constexpr int S_UC = 4288;
constexpr int S_RV = 4640;
constexpr int S_CH = 4 * S_UC + 2 * S_RV;
constexpr int S_KD = 4 * S_CH;
constexpr int LAGMAX = 2064;

__device__ __forceinline__ void cexp2(float a2, float brev, float L, float& re, float& im) {
  float mag = __builtin_amdgcn_exp2f(a2 * L);
  float ph = __builtin_amdgcn_fractf(brev * L);
  re = mag * __builtin_amdgcn_cosf(ph); im = mag * __builtin_amdgcn_sinf(ph);
}

__device__ __forceinline__ void ssm_unit(const Params& p, LAS unsigned char* lds, rsrc_t rs, int d0) {
  const int tid = threadIdx.x, lane = tid & 63, w = __builtin_amdgcn_readfirstlane(tid >> 6);
  const int c16 = lane & 15, g = lane >> 4, ch = w >> 1, hw = w & 1, d = d0 + ch;
  for (int i = tid; i < 4 * S_CH / 16; i += 512) *(LAS v4u*)(lds + i * 16) = v4u{0u, 0u, 0u, 0u};
  v2u xv[4];
#pragma unroll
  for (int i = 0; i < 4; i++) xv[i] = *(const v2u*)(p.x + (size_t)(i * 512 + tid) * 512 + d0);
  wgbar();
#pragma unroll
  for (int i = 0; i < 4; i++) {
    const int l = i * 512 + tid;
#pragma unroll
    for (int c = 0; c < 4; c++) {
      unsigned short v = (unsigned short)((c & 2 ? xv[i].y : xv[i].x) >> (16 * (c & 1)));
#pragma unroll
      for (int r = 0; r < 4; r++) *(LAS unsigned short*)(lds + c * S_CH + r * S_UC + (l + 32 - r) * 2) = v;
    }
  }
  {
    const float dt = rbf(expf(bf2f(p.logdt[d])));
    float L1[3];
    L1[0] = (float)((c16 & 7) + 128 * (c16 >> 3));
    L1[1] = (c16 < 8) ? (float)(256 + 128 * (c16 >> 2) + 2 * (c16 & 3)) : (float)(512 + 128 * ((c16 - 8) >> 1) + 4 * (c16 & 1));
    L1[2] = (float)(1024 + 128 * (c16 < 9 ? c16 : 0));
    const float L2 = (float)(8 * c16);
    f4 acc[3] = {f4{0.f, 0.f, 0.f, 0.f}, f4{0.f, 0.f, 0.f, 0.f}, f4{0.f, 0.f, 0.f, 0.f}};
    const int n0 = d * 128 + hw * 64 + g * 16;
    v4u pr[6][2];
#pragma unroll
    for (int q = 0; q < 2; q++) {
      pr[0][q] = *(const v4u*)(p.Are + n0 + 8 * q); pr[1][q] = *(const v4u*)(p.Aim + n0 + 8 * q);
      pr[2][q] = *(const v4u*)(p.Bre + n0 + 8 * q); pr[3][q] = *(const v4u*)(p.Bim + n0 + 8 * q);
      pr[4][q] = *(const v4u*)(p.Cre + n0 + 8 * q); pr[5][q] = *(const v4u*)(p.Cim + n0 + 8 * q);
    }
#pragma unroll
    for (int m = 0; m < 16; m++) {
#define PRM(k) __uint_as_float((m & 1) ? (pr[k][m >> 3][(m >> 1) & 3] & 0xFFFF0000u) : (pr[k][m >> 3][(m >> 1) & 3] << 16))
      const float a = dt * PRM(0), b = dt * PRM(1);
      const float br = dt * PRM(2), bi = dt * PRM(3), cr = PRM(4), ci = PRM(5);
#undef PRM
      const float cbr = cr * br - ci * bi, cbi = cr * bi + ci * br;
      const float a2 = a * 1.4426950408889634f, brev = b * 0.15915494309189535f;
      float yr, yi; cexp2(a2, brev, L2, yr, yi);
#pragma unroll
      for (int t = 0; t < 3; t++) {
        float er, ei; cexp2(a2, brev, L1[t], er, ei);
        float xr = cbr * er - cbi * ei, xi = cbr * ei + cbi * er;
        acc[t] = __builtin_amdgcn_mfma_f32_16x16x4f32(xr, yr, acc[t], 0, 0, 0);
        acc[t] = __builtin_amdgcn_mfma_f32_16x16x4f32(-xi, yi, acc[t], 0, 0, 0);
      }
    }
    LAS float* kd = (LAS float*)(lds + S_KD + w * 3072);
#pragma unroll
    for (int t = 0; t < 3; t++)
#pragma unroll
      for (int i = 0; i < 4; i++) kd[(16 * t + 4 * g + i) * 16 + c16] = acc[t][i];
  }
  wgbar();
#pragma unroll
  for (int i = 0; i < 4; i++) {
    const int l = i * 512 + tid;
    const int lr = (int)rbf((float)l);
    int row, j;
    if (lr < 256) { j = (lr >> 3) & 15; row = (lr & 7) + 8 * (lr >> 7); }
    else if (lr < 512) { int x = lr - 256; j = (x & 127) >> 3; row = 16 + 4 * (x >> 7) + ((x & 7) >> 1); }
    else if (lr < 1024) { int x = lr - 512; j = (x & 127) >> 3; row = 24 + 2 * (x >> 7) + ((x & 7) >> 2); }
    else { int x = lr - 1024; j = (x & 127) >> 3; row = 32 + (x >> 7); }
#pragma unroll
    for (int c = 0; c < 4; c++) {
      const LAS float* kd = (const LAS float*)(lds + S_KD + c * 6144);
      float k = kd[row * 16 + j] + kd[768 + row * 16 + j];
      float hi = rbf(k);
      LAS unsigned char* rv = lds + c * S_CH + 4 * S_UC + (LAGMAX - l) * 2;
      *(LAS unsigned short*)(rv) = f2bfu(hi);
      *(LAS unsigned short*)(rv + S_RV) = f2bfu(k - hi);
    }
  }
  wgbar();
  f4 acc[4];
#pragma unroll
  for (int t = 0; t < 4; t++) acc[t] = f4{0.f, 0.f, 0.f, 0.f};
  const int A0 = hw, A1 = 3 - hw, A2 = 4 + hw, A3 = 7 - hw;
  const LAS unsigned char* ap = lds + ch * S_CH + 4 * S_UC + (LAGMAX - 16 * c16 + 224 + 8 * g) * 2;
  const LAS unsigned char* bp = lds + ch * S_CH + (c16 & 3) * S_UC + ((c16 & 12) + 224 + 8 * g + 32) * 2;
#define SSM_STEP(T, A) { const LAS v2u* q = (const LAS v2u*)(bp + 512 * (A) - 64 * n); v2u b0 = q[0], b1 = q[1]; v4u bf = {b0.x, b0.y, b1.x, b1.y}; \
    acc[T] = mfma16(ah, bf, acc[T]); acc[T] = mfma16(al, bf, acc[T]); }
  int n = 0;
  for (; n <= 8 * A0 + 8; n++) { v4u ah = lds16(ap - 64 * n), al = lds16(ap - 64 * n + S_RV); SSM_STEP(0, A0) SSM_STEP(1, A1) SSM_STEP(2, A2) SSM_STEP(3, A3) }
  for (; n <= 8 * A1 + 8; n++) { v4u ah = lds16(ap - 64 * n), al = lds16(ap - 64 * n + S_RV); SSM_STEP(1, A1) SSM_STEP(2, A2) SSM_STEP(3, A3) }
  for (; n <= 8 * A2 + 8; n++) { v4u ah = lds16(ap - 64 * n), al = lds16(ap - 64 * n + S_RV); SSM_STEP(2, A2) SSM_STEP(3, A3) }
  for (; n <= 8 * A3 + 8; n++) { v4u ah = lds16(ap - 64 * n), al = lds16(ap - 64 * n + S_RV); SSM_STEP(3, A3) }
#undef SSM_STEP
  {
    const float dsk = bf2f(p.Dsk[d]);
#pragma unroll
    for (int t = 0; t < 4; t++) {
      const int A = t == 0 ? A0 : t == 1 ? A1 : t == 2 ? A2 : A3;
#pragma unroll
      for (int i = 0; i < 4; i++) {
        const int l = 256 * A + 16 * (4 * g + i) + c16;
        float u = bf2f(*(const LAS unsigned short*)(lds + ch * S_CH + (l + 32) * 2));
        float b = rbf(acc[t][i]) + rbf(u * dsk);
        *(LAS unsigned short*)(lds + S_KD + l * 8 + ch * 2) = f2bfu(b);
      }
    }
  }
  wgbar();
#pragma unroll
  for (int i = 0; i < 4; i++) {
    const int l = i * 512 + tid;
    st8(rs, OFF_YB + (unsigned)(l * 1024 + d0 * 2), *(const LAS v2u*)(lds + S_KD + l * 8));
  }
}

__device__ __forceinline__ void gate_unit(const Params& p, rsrc_t rs, int r0) {
  const int tid = threadIdx.x, lane = tid & 63, w = tid >> 6;
  v4u gw = *(const v4u*)(p.gw + lane * 8);
  for (int i = 0; i < 8; i++) {
    const int row = r0 + w * 8 + i;
    v4u xv = *(const v4u*)(p.x + (size_t)row * 512 + lane * 8);
    float s = 0.f;
#pragma unroll
    for (int k = 0; k < 4; k++) {
      s += rbf(__uint_as_float(xv[k] << 16) * __uint_as_float(gw[k] << 16));
      s += rbf(__uint_as_float(xv[k] & 0xFFFF0000u) * __uint_as_float(gw[k] & 0xFFFF0000u));
    }
#pragma unroll
    for (int o = 32; o > 0; o >>= 1) s += __shfl_xor(s, o);
    s = rbf(s);
    float gg = rbf(1.f / (1.f + expf(-s)));
    if (lane == 0) st4(rs, OFF_G + (unsigned)row * 4u, __float_as_uint(gg));
  }
}

__device__ __forceinline__ void out_unit(const Params& p, LAS unsigned char* lds, rsrc_t rs, int s, int cg, bool& dead) {
  const int tid = threadIdx.x, lane = tid & 63, w = __builtin_amdgcn_readfirstlane(tid >> 6);
  const int c16 = lane & 15, g = lane >> 4;
  const unsigned sb = OFF_STEP + (unsigned)s * STEP_BYTES;
  if (w == 0) wait_flags<32>(rs, OFF_UXF + (unsigned)(s * 32 + (lane & 31)) * 4u, dead);
  wgbar();
  v4u ux[4][4];
#pragma unroll
  for (int ct = 0; ct < 4; ct++)
#pragma unroll
    for (int i = 0; i < 4; i++) ux[ct][i] = ld16(rs, OFF_UX + (unsigned)s * 1048576u + (unsigned)(((4 * cg + ct) * 32 + 4 * w + i) * 1024 + lane * 16));
  const int erow = tid >> 4, ecol = 64 * cg + 4 * (tid & 15);
  const v2u yb = ld8(rs, OFF_YB + (unsigned)((32 * s + erow) * 1024 + ecol * 2));
  const float gg = __uint_as_float(ld4(rs, OFF_G + (unsigned)(32 * s + erow) * 4u));
  if (w == 0) {
    if (s < 62) wait_flags<32>(rs, OFF_PXF + (unsigned)(s * 64 + lane) * 4u, dead);
    else wait_flags<6>(rs, OFF_PXF + (unsigned)(s * 64 + lane) * 4u, dead);
  }
  wgbar();
  const unsigned xb = OFF_PX + (unsigned)s * 81920u;
  v4u pa[2][5];
#pragma unroll
  for (int rt = 0; rt < 2; rt++) {
#pragma unroll
    for (int i = 0; i < 4; i++) pa[rt][i] = ld16(rs, xb + (unsigned)(((4 * w + i) * 2 + rt) * 1024 + lane * 16));
    pa[rt][4] = ld16(rs, xb + 65536u + (unsigned)((w * 2 + rt) * 1024 + lane * 16));
  }
  v4u um[4];
  {
    unsigned o[4];
#pragma unroll
    for (int ct = 0; ct < 4; ct++) o[ct] = sb + ST_UM + (unsigned)((((4 * cg + ct) * 32 + 4 * w + g) * 16 + c16) * 16);
    poll16<4>(rs, o, um, dead);
  }
#pragma unroll
  for (int rt = 0; rt < 2; rt++)
#pragma unroll
    for (int ct = 0; ct < 4; ct++) {
      f4 acc = {0.f, 0.f, 0.f, 0.f};
#pragma unroll
      for (int i = 0; i < 4; i++) acc = mfma16(pa[rt][i], ux[ct][i], acc);
      acc = mfma16(pa[rt][4], um[ct], acc);
      *(LAS f4*)(lds + w * 8192 + (rt * 4 + ct) * 1024 + lane * 16) = acc;
    }
  wgbar();
  {
    f4 t = *(const LAS f4*)(lds + tid * 16);
#pragma unroll
    for (int k = 1; k < 8; k++) t += *(const LAS f4*)(lds + k * 8192 + tid * 16);
    const int rt = w >> 2, ct = w & 3;
    LAS float* at = (LAS float*)(lds + 65536);
#pragma unroll
    for (int i = 0; i < 4; i++) at[(16 * rt + 4 * g + i) * 68 + 16 * ct + c16] = t[i];
  }
  wgbar();
  {
    f4 a = *(const LAS f4*)(lds + 65536 + (erow * 68 + 4 * (tid & 15)) * 4);
    const float og = rbf(1.f - gg);
    float b0 = __uint_as_float(yb.x << 16), b1 = __uint_as_float(yb.x & 0xFFFF0000u);
    float b2 = __uint_as_float(yb.y << 16), b3 = __uint_as_float(yb.y & 0xFFFF0000u);
    float o0 = rbf(gg * rbf(a[0])) + rbf(og * b0), o1 = rbf(gg * rbf(a[1])) + rbf(og * b1);
    float o2 = rbf(gg * rbf(a[2])) + rbf(og * b2), o3 = rbf(gg * rbf(a[3])) + rbf(og * b3);
    *(v2u*)(p.out + (size_t)(32 * s + erow) * 512 + ecol) = v2u{pk2(o0, o1), pk2(o2, o3)};
  }
}

__device__ __forceinline__ void role_spare(const Params& p, LAS unsigned char* lds, rsrc_t rs, int j) {
  const int tid = threadIdx.x, w = __builtin_amdgcn_readfirstlane(tid >> 6);
  bool dead = false;
  const v4u sv = {SENT, SENT, SENT, SENT};
  STAMPR(0);
  constexpr unsigned N0 = INIT0_STEPS * STEP_BYTES / 16, N1 = 64u * STEP_BYTES / 16;
  warm_weights(p, j & 7, 4 + (j >> 3));
  for (unsigned i = (unsigned)(j * 512 + tid); i < N0; i += NSPARE * 512) st16(rs, OFF_STEP + i * 16u, sv);
  drain(); wgbar();
  if (tid == 0) st4(rs, OFF_CTR + CTR_INIT0 + (unsigned)j * 4u, 1u);
  STAMPR(1);
  for (unsigned i = N0 + (unsigned)(j * 512 + tid); i < N1; i += NSPARE * 512) st16(rs, OFF_STEP + i * 16u, sv);
  drain(); wgbar();
  if (tid == 0) st4(rs, OFF_CTR + CTR_INIT1 + (unsigned)j * 4u, 1u);
  STAMPR(2);
  if (j < 128) ssm_unit(p, lds, rs, 4 * j); else gate_unit(p, rs, 64 * (j - 128));
  drain(); wgbar();
  if (tid == 0) st4(rs, OFF_CTR + CTR_SSM + (unsigned)j * 4u, 1u);
  STAMPR(3);
  if (w == 0) { wait_ctr(rs, OFF_CTR + CTR_SSM, dead); wait_ctr(rs, OFF_CTR + CTR_INIT1, dead); }
  wgbar();
  STAMPR(4);
  for (int u = j; u < 512; u += NSPARE) { out_unit(p, lds, rs, u >> 3, u & 7, dead); STAMPR(5); }
}

__global__ void __launch_bounds__(512, 2) fused_kernel(Params p) {
  extern __shared__ __attribute__((aligned(16))) unsigned char smem[];
  LAS unsigned char* lds = (LAS unsigned char*)smem;
  const rsrc_t rs = __builtin_amdgcn_make_buffer_rsrc((void*)p.ws, 0, (int)WS_BYTES, 0x00020000);
  const int b = blockIdx.x;
  if (b < 32) role_qk(p, lds, rs, b);
  else if (b < 64) role_vo(p, lds, rs, b - 32);
  else if (b < 96) role_r(p, lds, rs, b - 64);
  else role_spare(p, lds, rs, b - 96);
}

extern "C" void kernel_launch(void* const* d_in, const int* in_sizes, int n_in, void* d_out, int out_size,
                              void* d_ws, size_t ws_size, hipStream_t stream) {
  Params p;
  p.x = (const unsigned short*)d_in[0]; p.mem0 = (const unsigned short*)d_in[1];
  p.Wq = (const unsigned short*)d_in[2]; p.Wk = (const unsigned short*)d_in[3];
  p.Wv = (const unsigned short*)d_in[4]; p.Wo = (const unsigned short*)d_in[5];
  p.Are = (const unsigned short*)d_in[8]; p.Aim = (const unsigned short*)d_in[9];
  p.Bre = (const unsigned short*)d_in[10]; p.Bim = (const unsigned short*)d_in[11];
  p.Cre = (const unsigned short*)d_in[12]; p.Cim = (const unsigned short*)d_in[13];
  p.logdt = (const unsigned short*)d_in[14]; p.Dsk = (const unsigned short*)d_in[15];
  p.gw = (const unsigned short*)d_in[16];
  p.out = (unsigned short*)d_out; p.ws = (unsigned char*)d_ws;
  (void)hipFuncSetAttribute((const void*)fused_kernel, hipFuncAttributeMaxDynamicSharedMemorySize, LDS_BYTES);
  (void)hipMemsetAsync(d_ws, 0, CTR_BYTES, stream);
  fused_kernel<<<256, 512, LDS_BYTES, stream>>>(p);
}
```

```cpp
#include <hip/hip_runtime.h>
#include <hip/hip_bf16.h>
#include <stdint.h>

typedef unsigned v4u __attribute__((ext_vector_type(4)));
typedef unsigned v2u __attribute__((ext_vector_type(2)));
typedef float f4 __attribute__((ext_vector_type(4)));
typedef float f2 __attribute__((ext_vector_type(2)));
typedef __bf16 bf8 __attribute__((ext_vector_type(8)));
typedef __bf16 bf16x16 __attribute__((ext_vector_type(16)));
typedef __bf16 bf2 __attribute__((ext_vector_type(2)));
typedef short s4 __attribute__((ext_vector_type(4)));
typedef unsigned v8u __attribute__((ext_vector_type(8)));
#define LAS __attribute__((address_space(3)))
typedef __amdgpu_buffer_rsrc_t rsrc_t;

constexpr unsigned SENT = 0xFFFFFFFFu;
#define AUXL 16
#define AUXS 16
constexpr int AUX_LD = AUXL, AUX_ST = AUXS;
constexpr unsigned OFF_CTR = 0;
constexpr unsigned CTR_INIT0 = 0, CTR_INIT1 = 1024, CTR_SSM = 2048;
#define FSB 16
constexpr unsigned FS = FSB;
constexpr unsigned OFF_UXF = 4096;
constexpr unsigned OFF_PXF = OFF_UXF + 2048u * FS;
constexpr unsigned CTR_BYTES = OFF_PXF + 4096u * FS;
constexpr unsigned OFF_STEP = 1048576;
constexpr unsigned ST_MEM = 0;
constexpr unsigned ST_P = 65536;
constexpr unsigned ST_UM = 65536 + 163840;
constexpr unsigned STEP_BYTES = 65536 + 163840 + 262144;
constexpr unsigned OFF_UX = OFF_STEP + 64u * STEP_BYTES;
constexpr unsigned OFF_PX = OFF_UX + 64u * 1048576u;
constexpr unsigned OFF_YB = OFF_PX + 64u * 81920u;
constexpr unsigned OFF_G = OFF_YB + 2097152u;
constexpr unsigned WS_BYTES = OFF_G + 8192u;
#define I0S 6
#define DLY1 30000
#define WARM 1
constexpr int INIT0_STEPS = I0S;
constexpr int NSPARE = 160;
constexpr unsigned SPIN_CAP = 400000u;
constexpr int LDS_BYTES = 136 * 1024;

#define PROF_DECL
#define PROF_END
#define STAMP(role, s, k) do {} while (0)
#define STAMPR(k) do {} while (0)
struct Params {
  const unsigned short* x; const unsigned short* mem0; const unsigned short* Wq; const unsigned short* Wk;
  const unsigned short* Wv; const unsigned short* Wo; const unsigned short* Are; const unsigned short* Aim;
  const unsigned short* Bre; const unsigned short* Bim; const unsigned short* Cre; const unsigned short* Cim;
  const unsigned short* logdt; const unsigned short* Dsk; const unsigned short* gw;
  unsigned short* out; unsigned char* ws;
};

__device__ __forceinline__ float bf2f(unsigned short u) { return __uint_as_float((unsigned)u << 16); }
__device__ __forceinline__ unsigned pk2(float lo, float hi) {
  bf2 v; v[0] = (__bf16)lo; v[1] = (__bf16)hi; return __builtin_bit_cast(unsigned, v);
}
__device__ __forceinline__ float rbf(float f) { return (float)(__bf16)f; }
__device__ __forceinline__ unsigned short f2bfu(float f) { return __builtin_bit_cast(unsigned short, (__bf16)f); }
__device__ __forceinline__ unsigned san(unsigned u) { return u < 0xFFFFFFFEu ? u : 0xFFFFFFFEu; }
__device__ __forceinline__ v4u ld16(rsrc_t rs, unsigned off) { return __builtin_amdgcn_raw_buffer_load_b128(rs, off, 0, AUX_LD); }
__device__ __forceinline__ unsigned ld4(rsrc_t rs, unsigned off) { return __builtin_amdgcn_raw_buffer_load_b32(rs, off, 0, AUX_LD); }
__device__ __forceinline__ v2u ld8(rsrc_t rs, unsigned off) { return __builtin_amdgcn_raw_buffer_load_b64(rs, off, 0, AUX_LD); }
__device__ __forceinline__ void st16(rsrc_t rs, unsigned off, v4u v) { __builtin_amdgcn_raw_buffer_store_b128(v, rs, off, 0, AUX_ST); }
__device__ __forceinline__ void st8(rsrc_t rs, unsigned off, v2u v) { __builtin_amdgcn_raw_buffer_store_b64(v, rs, off, 0, AUX_ST); }
__device__ __forceinline__ void st4(rsrc_t rs, unsigned off, unsigned v) { __builtin_amdgcn_raw_buffer_store_b32(v, rs, off, 0, AUX_ST); }
__device__ __forceinline__ void cbar() { asm volatile("" ::: "memory"); }
__device__ __forceinline__ void drain() { asm volatile("s_waitcnt vmcnt(0)" ::: "memory"); }
__device__ __forceinline__ void wgbar() {
  asm volatile("s_waitcnt lgkmcnt(0)" ::: "memory"); __builtin_amdgcn_s_barrier(); asm volatile("" ::: "memory");
}
__device__ __forceinline__ unsigned mx4(v4u v) { return max(max(v.x, v.y), max(v.z, v.w)); }
__device__ __forceinline__ v4u lds16(const LAS unsigned char* p) { return *(const LAS v4u*)p; }
__device__ __forceinline__ v2u ldstr(const LAS unsigned char* p) {
  return __builtin_bit_cast(v2u, __builtin_amdgcn_ds_read_tr16_b64_v4i16((LAS s4*)p));
}
__device__ __forceinline__ bf8 asbf8(v4u v) { return __builtin_bit_cast(bf8, v); }
__device__ __forceinline__ f4 mfma16(v4u a, v4u b, f4 c) { return __builtin_amdgcn_mfma_f32_16x16x32_bf16(asbf8(a), asbf8(b), c, 0, 0, 0); }
__device__ __forceinline__ f4 smf(v4u a, v4u b0, v4u b1, f4 c, int idx) {
  v8u b = {b0.x, b0.y, b0.z, b0.w, b1.x, b1.y, b1.z, b1.w};
  return __builtin_amdgcn_smfmac_f32_16x16x64_bf16(asbf8(a), __builtin_bit_cast(bf16x16, b), c, idx, 0, 0);
}
__device__ __forceinline__ float rmax(float a, float b) { float r; asm("v_max_f32 %0, %1, %2" : "=v"(r) : "v"(a), "v"(b)); return r; }
__device__ __forceinline__ float rmax3(float a, float b, float c) { float r; asm("v_max3_f32 %0, %1, %2, %3" : "=v"(r) : "v"(a), "v"(b), "v"(c)); return r; }
__device__ __forceinline__ float x32(float v) {
  unsigned u = __float_as_uint(v);
  auto r = __builtin_amdgcn_permlane32_swap(u, u, false, false);
  return __uint_as_float((threadIdx.x & 32) ? r[0] : r[1]);
}
__device__ __forceinline__ float add32(float v) { unsigned u = __float_as_uint(v); auto r = __builtin_amdgcn_permlane32_swap(u, u, false, false); return __uint_as_float(r[0]) + __uint_as_float(r[1]); }
__device__ __forceinline__ float max32(float v) { unsigned u = __float_as_uint(v); auto r = __builtin_amdgcn_permlane32_swap(u, u, false, false); return rmax(__uint_as_float(r[0]), __uint_as_float(r[1])); }
__device__ __forceinline__ float add16(float v) { unsigned u = __float_as_uint(v); auto r = __builtin_amdgcn_permlane16_swap(u, u, false, false); return __uint_as_float(r[0]) + __uint_as_float(r[1]); }
__device__ __forceinline__ float max16(float v) { unsigned u = __float_as_uint(v); auto r = __builtin_amdgcn_permlane16_swap(u, u, false, false); return rmax(__uint_as_float(r[0]), __uint_as_float(r[1])); }
__device__ __forceinline__ float addror8(float v) {
  int r = __builtin_amdgcn_update_dpp(0, __float_as_int(v), 0x128, 0xF, 0xF, false);
  return v + __int_as_float(r);
}
__device__ __forceinline__ float fold2(float a, float b) {
  auto r = __builtin_amdgcn_permlane32_swap(__float_as_uint(a), __float_as_uint(b), false, false);
  return __uint_as_float(r[0]) + __uint_as_float(r[1]);
}

template <int SL = 8>
__device__ __forceinline__ void wait_ctr(rsrc_t rs, unsigned off, bool& dead) {
  unsigned spins = dead ? SPIN_CAP : 0;
  const unsigned lane = threadIdx.x & 63;
  while (true) {
    cbar();
    v4u v = ld16(rs, off + (lane < NSPARE / 4 ? lane : 0u) * 16u);
    if (__all(min(min(v.x, v.y), min(v.z, v.w)) != 0u)) { cbar(); return; }
    if (++spins > SPIN_CAP) { dead = true; return; }
    __builtin_amdgcn_s_sleep(SL);
  }
}
template <int SL>
__device__ __forceinline__ void wait_flags(rsrc_t rs, unsigned off, bool& dead) {
  unsigned spins = dead ? SPIN_CAP : 0;
  while (true) {
    cbar();
    unsigned v = ld4(rs, off);
    if (__all(v != 0u)) { cbar(); return; }
    if (++spins > SPIN_CAP) { dead = true; return; }
    __builtin_amdgcn_s_sleep(SL > 127 ? 127 : SL);
    if (SL > 127) __builtin_amdgcn_s_sleep(SL > 254 ? 127 : SL - 127);
  }
}
__device__ __forceinline__ unsigned now32() { return (unsigned)__builtin_amdgcn_s_memtime(); }
__device__ __forceinline__ void pace(unsigned tref, unsigned d) {
  for (int i = 0; i < 256; i++) {
    const int rem = (int)(tref + d - now32());
    if (rem <= 0 || rem > 20000) return;
    if (rem > 640) __builtin_amdgcn_s_sleep(8); else if (rem > 192) __builtin_amdgcn_s_sleep(2);
  }
}
#define DW 0
#define DQ 3050
#define DV 3650
#define DR 3950
template <int C>
__device__ __forceinline__ void poll16(rsrc_t rs, const unsigned (&off)[C], v4u (&v)[C], bool& dead) {
  unsigned spins = dead ? SPIN_CAP : 0;
  while (true) {
    cbar();
#pragma unroll
    for (int c = 0; c < C; c++) v[c] = ld16(rs, off[c]);
    unsigned m = 0;
#pragma unroll
    for (int c = 0; c < C; c++) m = max(m, mx4(v[c]));
    if (__all(m != SENT)) return;
    if (++spins > SPIN_CAP) { dead = true; return; }
  }
}

template <int NCT>
__device__ __forceinline__ void wload_issue(const unsigned short* src, int rowstride, int lane, v4u (&t)[NCT * 2]) {
  constexpr int LPR = NCT * 2;
  constexpr int RPI = 64 / LPR;
#pragma unroll
  for (int i = 0; i < NCT * 2; i++) {
    int row = i * RPI + lane / LPR, pc = lane % LPR;
    t[i] = *(const v4u*)(src + (size_t)row * rowstride + pc * 8);
  }
}
template <int NCT>
__device__ __forceinline__ void wload_finish(LAS unsigned char* slab, int lane, const v4u (&t)[NCT * 2], v4u (&f)[NCT][2]) {
  constexpr int LPR = NCT * 2, RPI = 64 / LPR, RB = NCT * 32;
#pragma unroll
  for (int i = 0; i < NCT * 2; i++) {
    int row = i * RPI + lane / LPR, pc = lane % LPR;
    *(LAS v4u*)(slab + row * RB + pc * 16) = t[i];
  }
  asm volatile("s_waitcnt lgkmcnt(0)" ::: "memory");
  int gb = lane >> 4, q = (lane >> 2) & 3, p = lane & 3;
#pragma unroll
  for (int ct = 0; ct < NCT; ct++)
#pragma unroll
    for (int hf = 0; hf < 2; hf++) {
      v2u a = ldstr(slab + (32 * hf + 8 * gb + q) * RB + ct * 32 + p * 8);
      v2u b = ldstr(slab + (32 * hf + 8 * gb + 4 + q) * RB + ct * 32 + p * 8);
      f[ct][hf] = v4u{a.x, a.y, b.x, b.y};
    }
  asm volatile("s_waitcnt lgkmcnt(0)" ::: "memory");
}

constexpr int WROW = 272;
__device__ __forceinline__ v4u wrow_issue(const unsigned short* src, int row0, int tid) {
  return *(const v4u*)(src + (size_t)(row0 + (tid >> 4)) * 4096 + (tid & 15) * 8);
}
__device__ __forceinline__ void wrow_store(LAS unsigned char* img, int row0, int tid, v4u v) {
  *(LAS v4u*)(img + (row0 + (tid >> 4)) * WROW + (tid & 15) * 16) = v;
}
__device__ __forceinline__ v4u wrow_frag(const LAS unsigned char* img, int row0, int colbyte, int lane) {
  const int gb = lane >> 4, q = (lane >> 2) & 3, p = lane & 3;
  v2u a = ldstr(img + (row0 + 8 * gb + q) * WROW + colbyte + p * 8);
  v2u b = ldstr(img + (row0 + 8 * gb + 4 + q) * WROW + colbyte + p * 8);
  return v4u{a.x, a.y, b.x, b.y};
}
constexpr int L_XS = 40960;
constexpr int L_MEM = 0;
constexpr int L_QKT = 8192;
constexpr int L_QXT = 16384;
constexpr int L_KXT = 24576;
constexpr int L_KXP = 32768;
constexpr int L_SLAB = 0;

__device__ __forceinline__ void x_dma(const Params& p, LAS unsigned char* lds, int s, int w, int lane) {
#pragma unroll
  for (int i = 0; i < 4; i++) {
    const int r = 4 * w + i;
    const unsigned short* src = p.x + (size_t)(s * 32 + r) * 512 + (lane ^ (r & 15)) * 8;
    const unsigned dst = __builtin_amdgcn_readfirstlane((unsigned)(uintptr_t)(lds + L_XS + (s & 1) * 32768 + r * 1024));
    unsigned keep;
    asm volatile("s_mov_b32 %0, m0\n\ts_mov_b32 m0, %2\n\ts_nop 0\n\tglobal_load_lds_dwordx4 %1, off\n\ts_mov_b32 m0, %0"
                 : "=&s"(keep) : "v"(src), "s"(dst) : "memory");
  }
}
__device__ __forceinline__ v4u x_frag(const LAS unsigned char* lds, int s, int rt, int ks, int c16, int g) {
  return lds16(lds + ((s & 1) * 32768 + c16 * 1024 + ((4 * (ks & 3) + g) ^ c16) * 16) + (L_XS + rt * 16384 + (ks >> 2) * 256));
}
__device__ __forceinline__ int t64(int e, int gam) { return e * 64 + ((gam ^ (e >> 1)) & 7) * 8; }
__device__ __forceinline__ v4u t64_frag(const LAS unsigned char* img, int ks, int half, int g, int tq, int tp) {
  v2u a = ldstr(img + ks * 2048 + t64(8 * g + tq, 4 * half + tp));
  v2u b = ldstr(img + ks * 2048 + t64(8 * g + 4 + tq, 4 * half + tp));
  return v4u{a.x, a.y, b.x, b.y};
}
__device__ __forceinline__ v4u t16_frag(const LAS unsigned char* img, int ks, int g, int tq, int tp) {
  const int o = (8 * g + tq) * 16 + ((tp ^ g) & 1) * 8;
  v2u a = ldstr(img + ks * 512 + o);
  v2u b = ldstr(img + ks * 512 + 64 + o);
  return v4u{a.x, a.y, b.x, b.y};
}
__device__ __forceinline__ v4u mem0_chunk(const Params& p, int w, int lane) {
  int r = lane & 15, ga = lane >> 4;
  const unsigned* m = (const unsigned*)(p.mem0 + (r & 7) * 512 + 64 * w + 16 * ga + 2 * (r >> 3));
  return v4u{m[0], m[2], m[4], m[6]};
}
#define RW1 1
#define RC 1
#define DX 0

constexpr float SCL2 = 0.08838834764831845f * 1.4426950408889634f;

template <int CLS>
__device__ __forceinline__ void qk_loop(const Params& p, LAS unsigned char* lds, rsrc_t rs, int h, const v4u (&Wf)[8][2][2], v4u m0, bool dead) {
  const int tid = threadIdx.x, lane = tid & 63, w = __builtin_amdgcn_readfirstlane(tid >> 6);
  const int c16 = lane & 15, g = lane >> 4, tq = (lane >> 2) & 3, tp = lane & 3;
  const int idx = (c16 < 8) ? 0x4444 : 0xEEEE;
  const unsigned cp = (unsigned)(blockIdx.x & 7);
  PROF_DECL
  for (int s = 0; s < 64; s++) {
    STAMP(blockIdx.x, s, 0);
    const unsigned tref = now32();
    {
      f4 acc[2][2];
#pragma unroll
      for (int rt = 0; rt < 2; rt++)
#pragma unroll
        for (int ct = 0; ct < 2; ct++) acc[rt][ct] = f4{0.f, 0.f, 0.f, 0.f};
      if (s < 63) x_dma(p, lds, s + 1, w, lane);
      v4u a[2][2][2];
#pragma unroll
      for (int k = 0; k < 2; k++)
#pragma unroll
        for (int rt = 0; rt < 2; rt++) a[0][k][rt] = x_frag(lds, s, rt, k, c16, g);
#pragma unroll
      for (int kk = 0; kk < 8; kk++) {
        if (kk < 7) {
#pragma unroll
          for (int k = 0; k < 2; k++)
#pragma unroll
            for (int rt = 0; rt < 2; rt++) a[(kk + 1) & 1][k][rt] = x_frag(lds, s, rt, 2 * kk + 2 + k, c16, g);
        }
        __builtin_amdgcn_sched_barrier(0);
#pragma unroll
        for (int k = 0; k < 2; k++)
#pragma unroll
          for (int rt = 0; rt < 2; rt++)
#pragma unroll
            for (int ct = 0; ct < 2; ct++) acc[rt][ct] = mfma16(a[kk & 1][k][rt], Wf[kk][ct][k], acc[rt][ct]);
        __builtin_amdgcn_sched_barrier(0);
      }
#pragma unroll
      for (int rt = 0; rt < 2; rt++)
#pragma unroll
        for (int ct = 0; ct < 2; ct++) {
          int e = 32 * (w & 3) + 16 * ct + c16;
          unsigned p0 = pk2(acc[rt][ct][0], acc[rt][ct][1]), p1 = pk2(acc[rt][ct][2], acc[rt][ct][3]);
          *(LAS v2u*)(lds + (CLS < 2 ? L_QXT : L_KXT) + t64(e, 4 * rt + g)) = v2u{p0, p1};
          if (CLS == 2) {
            const int gam = (g & 1) * 4 + (g >> 1) * 2;
            *(LAS unsigned*)(lds + L_KXP + t64(e, gam) + rt * 4) = p0;
            *(LAS unsigned*)(lds + L_KXP + t64(e, gam + 1) + rt * 4) = p1;
          }
        }
    }
    STAMP(blockIdx.x, s, 6);
    wgbar();
    v4u fr[8];
    if (CLS == 0) {
#pragma unroll
      for (int tau = 0; tau < 2; tau++)
#pragma unroll
        for (int ks = 0; ks < 4; ks++) fr[tau * 4 + ks] = t64_frag(lds + L_KXP, ks, tau, g, tq, tp);
    } else if (CLS == 1) {
      const int rt = w - 2;
      f4 sxx[2] = {f4{0.f, 0.f, 0.f, 0.f}, f4{0.f, 0.f, 0.f, 0.f}};
      v4u kf[2][4];
#pragma unroll
      for (int ks = 0; ks < 4; ks++) {
        fr[ks] = t64_frag(lds + L_QXT, ks, rt, g, tq, tp);
        kf[0][ks] = t64_frag(lds + L_KXT, ks, 0, g, tq, tp);
        kf[1][ks] = t64_frag(lds + L_KXT, ks, 1, g, tq, tp);
      }
#pragma unroll
      for (int ks = 0; ks < 4; ks++)
#pragma unroll
        for (int tau = 0; tau < 2; tau++) sxx[tau] = mfma16(kf[tau][ks], fr[ks], sxx[tau]);
      fr[4] = __builtin_bit_cast(v4u, sxx[0]); fr[5] = __builtin_bit_cast(v4u, sxx[1]);
    } else if (w == 7 && s == INIT0_STEPS - 1) {
      wait_ctr(rs, OFF_CTR + CTR_INIT1, dead);
    }
    const unsigned sb = OFF_STEP + (unsigned)s * STEP_BYTES;
    STAMP(blockIdx.x, s, 1);
    {
      v4u m[1];
      if (s == 0) m[0] = m0;
      else { const unsigned o[1] = {sb + ST_MEM + cp * 8192u + (unsigned)tid * 16u}; pace(tref, DQ); poll16<1>(rs, o, m, dead); }
      STAMP(blockIdx.x, s, 2);
      *(LAS v4u*)(lds + L_MEM + tid * 16) = m[0];
      drain();
    }
    wgbar();
    STAMP(blockIdx.x, s, 3);
    {
      f4 acc[2] = {f4{0.f, 0.f, 0.f, 0.f}, f4{0.f, 0.f, 0.f, 0.f}};
      v4u a[8];
#pragma unroll
      for (int t = 0; t < 8; t++) a[t] = lds16(lds + L_MEM + t * 1024 + lane * 16);
      __builtin_amdgcn_sched_barrier(0);
#pragma unroll
      for (int t = 0; t < 8; t++)
#pragma unroll
        for (int ct = 0; ct < 2; ct++) acc[ct] = smf(a[t], Wf[t][ct][0], Wf[t][ct][1], acc[ct], idx);
      float f[4];
#pragma unroll
      for (int i = 0; i < 4; i++) f[i] = fold2(acc[0][i], acc[1][i]);
      *(LAS v2u*)(lds + L_QKT + (32 * w + 16 * (g >> 1) + c16) * 16 + ((g ^ (c16 >> 3)) & 1) * 8) = v2u{pk2(f[0], f[1]), pk2(f[2], f[3])};
    }
    wgbar();
    STAMP(blockIdx.x, s, 4);
    if (CLS == 0) {
      if (s < 63) {
        f4 sx0 = {0.f, 0.f, 0.f, 0.f}, sx1 = sx0, sm = sx0;
        v4u qf[4], kf[4];
#pragma unroll
        for (int ks = 0; ks < 4; ks++) { qf[ks] = t16_frag(lds + L_QKT, ks, g, tq, tp); kf[ks] = t16_frag(lds + L_QKT + 2048, ks, g, tq, tp); }
        __builtin_amdgcn_sched_barrier(0);
#pragma unroll
        for (int ks = 0; ks < 4; ks++) {
          sx0 = mfma16(fr[ks], qf[ks], sx0);
          sm = mfma16(kf[ks], qf[ks], sm);
          sx1 = mfma16(fr[4 + ks], qf[ks], sx1);
          __builtin_amdgcn_sched_barrier(0);
        }
        const bool hi = c16 >= 8;
        float v[6];
#pragma unroll
        for (int i = 0; i < 4; i++) v[i] = hi ? sx1[i] : sx0[i];
        v[4] = hi ? sm[2] : sm[0]; v[5] = hi ? sm[3] : sm[1];
        float mx = rmax3(rmax3(v[0], v[1], v[2]), rmax(v[3], v[4]), v[5]);
        mx = rmax(mx, __int_as_float(__builtin_amdgcn_update_dpp(__float_as_int(mx), __float_as_int(mx), 0x128, 0xF, 0xF, false)));
        mx = max32(max16(mx));
        const float nb = -mx * SCL2;
#pragma unroll
        for (int i = 0; i < 6; i++) v[i] = __builtin_amdgcn_exp2f(__builtin_fmaf(v[i], SCL2, nb));
        float sum = ((v[0] + v[1]) + (v[2] + v[3])) + 0.5f * (v[4] + v[5]);
        sum = add32(add16(addror8(sum)));
        float inv = __builtin_amdgcn_rcpf(sum);
        const int a0 = (int)san(pk2(v[0] * inv, v[1] * inv)), a1 = (int)san(pk2(v[2] * inv, v[3] * inv)), a2 = (int)san(pk2(v[4] * inv, v[5] * inv));
        v4u px = {(unsigned)__builtin_amdgcn_update_dpp(a0, a0, 0x128, 0xF, 0xC, false), (unsigned)__builtin_amdgcn_update_dpp(a1, a1, 0x128, 0xF, 0xC, false),
                  (unsigned)__builtin_amdgcn_update_dpp(a0, a0, 0x128, 0xF, 0x3, false), (unsigned)__builtin_amdgcn_update_dpp(a1, a1, 0x128, 0xF, 0x3, false)};
        v2u pm = {(unsigned)__builtin_amdgcn_update_dpp(a2, a2, 0x128, 0xF, 0xC, false), (unsigned)__builtin_amdgcn_update_dpp(a2, a2, 0x128, 0xF, 0x3, false)};
        const unsigned pb = sb + ST_P + (unsigned)(4 * w + (c16 >> 3)) * 20480u;
        const unsigned ox = (unsigned)(h * 512 + (g * 8 + (c16 & 7)) * 16);
        st16(rs, pb + ox, px);
        st16(rs, pb + 40960u + ox, px);
        st8(rs, pb + (unsigned)(g >> 1) * 40960u + 16384u + (unsigned)((((h >> 1) * 16) + (g & 1) * 8 + (c16 & 7)) * 16 + (h & 1) * 8), pm);
      }
    } else if (CLS == 1) {
      const int rt = w - 2;
      if (lane == 0 && s > 0) st4(rs, OFF_PXF + (unsigned)((s - 1) * 64 + h * 2 + rt) * FS, 1u);
      f4 sm = {0.f, 0.f, 0.f, 0.f};
      v4u kf[4];
#pragma unroll
      for (int ks = 0; ks < 4; ks++) kf[ks] = t16_frag(lds + L_QKT + 2048, ks, g, tq, tp);
#pragma unroll
      for (int ks = 0; ks < 4; ks++) sm = mfma16(kf[ks], fr[ks], sm);
      float v[12];
      const int R = 16 * rt + c16;
#pragma unroll
      for (int i = 0; i < 4; i++) {
        v[i] = (4 * g + i <= R) ? __uint_as_float(fr[4][i]) : -3.0e38f;
        v[4 + i] = (16 + 4 * g + i <= R) ? __uint_as_float(fr[5][i]) : -3.0e38f;
        v[8 + i] = sm[i];
      }
      float mx = v[0];
#pragma unroll
      for (int i = 1; i < 12; i++) mx = fmaxf(mx, v[i]);
      mx = max32(max16(mx));
      float sum = 0.f, summ = 0.f;
#pragma unroll
      for (int i = 0; i < 12; i++) {
        float e = __builtin_amdgcn_exp2f((v[i] - mx) * SCL2);
        if (i < 4) e = (4 * g + i <= R) ? e : 0.f; else if (i < 8) e = (12 + 4 * g + i <= R) ? e : 0.f;
        v[i] = e; if (i < 8) sum += e; else summ += e;
      }
      sum += 0.5f * summ;
      sum = add32(add16(sum));
      float inv = __builtin_amdgcn_rcpf(sum);
      v4u px = {san(pk2(v[0] * inv, v[1] * inv)), san(pk2(v[2] * inv, v[3] * inv)), san(pk2(v[4] * inv, v[5] * inv)), san(pk2(v[6] * inv, v[7] * inv))};
      unsigned pm = san((g < 2) ? pk2(v[8] * inv, v[9] * inv) : pk2(v[10] * inv, v[11] * inv));
      const unsigned xb = OFF_PX + (unsigned)s * 81920u;
      st16(rs, xb + (unsigned)((h * 2 + rt) * 1024 + lane * 16), px);
      st4(rs, xb + 65536u + (unsigned)((((h >> 2) * 2 + rt) * 64 + (h & 3) * 16 + c16) * 16 + g * 4), pm);
    }
    STAMP(blockIdx.x, s, 5);
    __builtin_amdgcn_s_barrier();
    if (s == 0) STAMPR(2);
  }
  PROF_END; STAMPR(3);
}

__device__ __forceinline__ void role_qk(const Params& p, LAS unsigned char* lds, rsrc_t rs, int h) {
  const int tid = threadIdx.x, lane = tid & 63, w = __builtin_amdgcn_readfirstlane(tid >> 6);
  bool dead = false;
  STAMPR(0);
  if (DW) pace(now32(), DW);
  v4u Wf[8][2][2];
  {
    v4u t[2][2][8];
#pragma unroll
    for (int r = 0; r < 2; r++)
#pragma unroll
      for (int i = 0; i < 8; i++) {
        t[r][0][i] = wrow_issue(p.Wq + h * 128, 256 * r + 32 * i, tid);
        t[r][1][i] = wrow_issue(p.Wk + h * 128, 256 * r + 32 * i, tid);
      }
#pragma unroll
    for (int r = 0; r < 2; r++) {
#pragma unroll
      for (int i = 0; i < 8; i++) { wrow_store(lds, 32 * i, tid, t[r][0][i]); wrow_store(lds + 256 * WROW, 32 * i, tid, t[r][1][i]); }
      wgbar();
#pragma unroll
      for (int k = 0; k < 4; k++)
#pragma unroll
        for (int ct = 0; ct < 2; ct++)
#pragma unroll
          for (int hf = 0; hf < 2; hf++)
            Wf[4 * r + k][ct][hf] = wrow_frag(lds + (w >> 2) * 256 * WROW, 64 * k + 32 * hf, (w & 3) * 64 + ct * 32, lane);
      wgbar();
    }
  }
  x_dma(p, lds, 0, w, lane);
  const v4u m0 = mem0_chunk(p, w, lane);
  if (w == 7) wait_ctr(rs, OFF_CTR + CTR_INIT0, dead);
  drain(); wgbar();

  STAMPR(1);
  if (w < 2) qk_loop<0>(p, lds, rs, h, Wf, m0, dead);
  else if (w < 4) qk_loop<1>(p, lds, rs, h, Wf, m0, dead);
  else qk_loop<2>(p, lds, rs, h, Wf, m0, dead);
}

__device__ __forceinline__ void role_vo(const Params& p, LAS unsigned char* lds, rsrc_t rs, int h) {
  const int tid = threadIdx.x, lane = tid & 63, w = __builtin_amdgcn_readfirstlane(tid >> 6);
  const int c16 = lane & 15, g = lane >> 4, tq = (lane >> 2) & 3, tp = lane & 3;
  const int idx = (c16 < 8) ? 0x4444 : 0xEEEE;
  const unsigned cp = (unsigned)(blockIdx.x & 7);
  bool dead = false;
  PROF_DECL STAMPR(0);
  if (DW) pace(now32(), DW);
  v4u Vf[8][1][2], Of[2][4][2];
  {
    const unsigned short* so = p.Wo + (size_t)h * 65536 + 64 * w;
    v4u t[16], t2[2][8];
#pragma unroll
    for (int i = 0; i < 16; i++) t[i] = wrow_issue(p.Wv + h * 128, 32 * i, tid);
#pragma unroll
    for (int k = 0; k < 2; k++) wload_issue<4>(so + (size_t)k * 64 * 512, 512, lane, t2[k]);
#pragma unroll
    for (int i = 0; i < 16; i++) wrow_store(lds, 32 * i, tid, t[i]);
    wgbar();
#pragma unroll
    for (int k = 0; k < 8; k++)
#pragma unroll
      for (int hf = 0; hf < 2; hf++) Vf[k][0][hf] = wrow_frag(lds, 64 * k + 32 * hf, w * 32, lane);
    wgbar();
#pragma unroll
    for (int k = 0; k < 2; k++) wload_finish<4>(lds + L_SLAB + w * 8192, lane, t2[k], Of[k]);
    wgbar();
  }
  x_dma(p, lds, 0, w, lane);
  const v4u m0 = mem0_chunk(p, w, lane);
  if (w == 7) wait_ctr(rs, OFF_CTR + CTR_INIT0, dead);
  drain(); wgbar();
  STAMPR(1);
  for (int sx = 0; sx < 65; sx++) {
    const int s = sx == 0 ? 0 : sx == 1 ? -1 : sx - 1;
    const unsigned tref = now32();
    if (sx < 64) {
      STAMP(blockIdx.x, s, 0);
      {
        f4 acc[2] = {f4{0.f, 0.f, 0.f, 0.f}, f4{0.f, 0.f, 0.f, 0.f}};
        if (sx < 63) x_dma(p, lds, sx + 1, w, lane);
        v4u a[3][2][2];
#pragma unroll
        for (int q = 0; q < 2; q++)
#pragma unroll
          for (int k = 0; k < 2; k++)
#pragma unroll
            for (int rt = 0; rt < 2; rt++) a[q][k][rt] = x_frag(lds, sx, rt, 2 * q + k, c16, g);
#pragma unroll
        for (int kk = 0; kk < 8; kk++) {
          if (kk < 6) {
#pragma unroll
            for (int k = 0; k < 2; k++)
#pragma unroll
              for (int rt = 0; rt < 2; rt++) a[(kk + 2) % 3][k][rt] = x_frag(lds, sx, rt, 2 * kk + 4 + k, c16, g);
          }
          __builtin_amdgcn_sched_barrier(0);
#pragma unroll
          for (int k = 0; k < 2; k++)
#pragma unroll
            for (int rt = 0; rt < 2; rt++) acc[rt] = mfma16(a[kk % 3][k][rt], Vf[kk][0][k], acc[rt]);
          __builtin_amdgcn_sched_barrier(0);
        }
#pragma unroll
        for (int rt = 0; rt < 2; rt++)
          *(LAS v2u*)(lds + L_QXT + t64(16 * w + c16, 4 * rt + g)) = v2u{pk2(acc[rt][0], acc[rt][1]), pk2(acc[rt][2], acc[rt][3])};
      }
      wgbar();
      {
        f4 acc[2][4];
#pragma unroll
        for (int rt = 0; rt < 2; rt++)
#pragma unroll
          for (int ct = 0; ct < 4; ct++) acc[rt][ct] = f4{0.f, 0.f, 0.f, 0.f};
        v4u af[4][2];
#pragma unroll
        for (int ks = 0; ks < 4; ks++)
#pragma unroll
          for (int rt = 0; rt < 2; rt++) af[ks][rt] = t64_frag(lds + L_QXT, ks, rt, g, tq, tp);
        __builtin_amdgcn_sched_barrier(0);
#pragma unroll
        for (int ks = 0; ks < 4; ks++)
#pragma unroll
          for (int rt = 0; rt < 2; rt++)
#pragma unroll
            for (int ct = 0; ct < 4; ct++) acc[rt][ct] = mfma16(af[ks][rt], Of[ks >> 1][ct][ks & 1], acc[rt][ct]);
#pragma unroll
        for (int ct = 0; ct < 4; ct++) {
          v4u u = {pk2(acc[0][ct][0], acc[0][ct][1]), pk2(acc[0][ct][2], acc[0][ct][3]), pk2(acc[1][ct][0], acc[1][ct][1]), pk2(acc[1][ct][2], acc[1][ct][3])};
          st16(rs, OFF_UX + (unsigned)sx * 1048576u + (unsigned)(((4 * w + ct) * 32 + h) * 1024 + lane * 16), u);
        }
      }
    }
    if (s < 0) {
      drain(); wgbar();
      if (tid == 0) st4(rs, OFF_UXF + (unsigned)(sx * 32 + h) * FS, 1u);
      continue;
    }
    if (w == 7 && s == INIT0_STEPS - 1) wait_ctr(rs, OFF_CTR + CTR_INIT1, dead);
    STAMP(blockIdx.x, s, 1);
    const unsigned sb = OFF_STEP + (unsigned)s * STEP_BYTES;
    {
      v4u m[1];
      if (s == 0) m[0] = m0;
      else { const unsigned o[1] = {sb + ST_MEM + cp * 8192u + (unsigned)tid * 16u}; pace(tref, DV); poll16<1>(rs, o, m, dead); }
      STAMP(blockIdx.x, s, 2);
      *(LAS v4u*)(lds + L_MEM + tid * 16) = m[0];
      if (s > 0) drain();
    }
    wgbar();
    if (tid == 64 && sx < 64 && s > 0) st4(rs, OFF_UXF + (unsigned)(sx * 32 + h) * FS, 1u);
    {
      f4 a0 = {0.f, 0.f, 0.f, 0.f}, a1 = a0;
      v4u a[8];
#pragma unroll
      for (int t = 0; t < 8; t++) a[t] = lds16(lds + L_MEM + t * 1024 + lane * 16);
      __builtin_amdgcn_sched_barrier(0);
#pragma unroll
      for (int t = 0; t < 8; t += 2) {
        a0 = smf(a[t], Vf[t][0][0], Vf[t][0][1], a0, idx);
        a1 = smf(a[t + 1], Vf[t + 1][0][0], Vf[t + 1][0][1], a1, idx);
      }
      a0 += a1;
      float f0 = fold2(a0[0], a0[1]), f1 = fold2(a0[2], a0[3]);
      const int gp = g & 1, up = g >> 1;
      const int hf = (c16 >> 1) & 1, j = 2 * (c16 >> 2) + (c16 & 1);
      LAS unsigned char* b = lds + L_QKT + (w * 16 + hf * 8 + 2 * gp + up) * 16 + j * 2;
      *(LAS unsigned short*)(b) = f2bfu(f0);
      *(LAS unsigned short*)(b + 64) = f2bfu(f1);
    }
    if (s == 0) drain();
    wgbar();
    if (s == 0 && tid == 64) st4(rs, OFF_UXF + (unsigned)h * FS, 1u);
    {
      v4u a0 = lds16(lds + L_QKT + lane * 16), a1 = lds16(lds + L_QKT + 1024 + lane * 16);
      f4 acc[4];
#pragma unroll
      for (int ct = 0; ct < 4; ct++) {
        acc[ct] = smf(a0, Of[0][ct][0], Of[0][ct][1], f4{0.f, 0.f, 0.f, 0.f}, idx);
        acc[ct] = smf(a1, Of[1][ct][0], Of[1][ct][1], acc[ct], idx);
      }
#pragma unroll
      for (int ct = 0; ct < 4; ct += 2) {
        float f[4];
#pragma unroll
        for (int i = 0; i < 4; i++) f[i] = fold2(acc[ct][i], acc[ct + 1][i]);
        v2u u = {san(pk2(f[0], f[1])), san(pk2(f[2], f[3]))};
        st8(rs, sb + ST_UM + (unsigned)((((4 * w + ct + (g >> 1)) * 32 + h) * 16 + c16) * 16 + (g & 1) * 8), u);
      }
    }
    STAMP(blockIdx.x, s, 5);
    if (s == 0) STAMPR(2);
  }
  PROF_END; STAMPR(3);
}

__device__ __forceinline__ void warm_weights(const Params& p, int cls, int q) {
  unsigned acc = 0;
#pragma unroll
  for (int k = 0; k < 2; k++) {
    const int i0 = q * 512 + (int)threadIdx.x + k * 24 * 512, i = i0 < 16384 ? i0 : i0 - 16384;
    const int line = i & 1, row = (i >> 1) & 511, m = (i >> 10) & 3, hh = cls + 8 * (i >> 12);
    const unsigned short* a = m == 3 ? p.Wo + (size_t)hh * 65536 + (row * 2 + line) * 64
                                     : (m == 0 ? p.Wq : m == 1 ? p.Wk : p.Wv) + (size_t)row * 4096 + hh * 128 + line * 64;
    acc += *(const unsigned*)a;
  }
  asm volatile("" :: "v"(acc));
}
__device__ __forceinline__ void role_r(const Params& p, LAS unsigned char* lds, rsrc_t rs, int c) {
  const int tid = threadIdx.x, lane = tid & 63, w = __builtin_amdgcn_readfirstlane(tid >> 6);
  const int c16 = lane & 15, g = lane >> 4;
  const int idx = (c16 < 8) ? 0x4444 : 0xEEEE;
  const unsigned cp = (unsigned)(blockIdx.x & 7);
  bool dead = false;
  PROF_DECL STAMPR(0);
  if (WARM) warm_weights(p, c & 7, c >> 3);
  if (c >= 32 / RC) return;
  const int c0 = RC * c;
  if (w == 7) wait_ctr(rs, OFF_CTR + CTR_INIT0, dead);
  wgbar();
  for (int s = 0; s < 63; s++) {
    const unsigned sb = OFF_STEP + (unsigned)s * STEP_BYTES;
    const unsigned tref = now32();
    if (s == INIT0_STEPS - 2) wait_ctr(rs, OFF_CTR + CTR_INIT1, dead);
    STAMP(blockIdx.x, s, 0);
    if (RW1) { if (w == 0) wait_flags<4>(rs, OFF_UXF + (unsigned)(s * 32 + (lane & 31)) * FS, dead); wgbar(); }
    else wait_flags<4>(rs, OFF_UXF + (unsigned)(s * 32 + (lane & 31)) * FS, dead);
    STAMP(blockIdx.x, s, 1);
    f4 acc[1] = {f4{0.f, 0.f, 0.f, 0.f}};
    const unsigned pb = sb + ST_P + cp * 20480u;
    const unsigned ub = OFF_UX + (unsigned)s * 1048576u + (unsigned)(c0 * 32768 + lane * 16);
    if (w < 4) {
      v4u ux[6], pa[3];
#pragma unroll
      for (int i = 0; i < 6; i++) ux[i] = ld16(rs, ub + (unsigned)((6 * w + i) * 1024));
      pace(tref, DR);
      STAMP(blockIdx.x, s, 2);
      const unsigned o[3] = {pb + (unsigned)(3 * w * 1024 + lane * 16), pb + (unsigned)((3 * w + 1) * 1024 + lane * 16), pb + (unsigned)((3 * w + 2) * 1024 + lane * 16)};
      poll16<3>(rs, o, pa, dead);
#pragma unroll
      for (int i = 0; i < 3; i++) acc[0] = smf(pa[i], ux[2 * i], ux[2 * i + 1], acc[0], idx);
    } else {
      const int v = w - 4;
      v4u ux[2], pa[4];
#pragma unroll
      for (int i = 0; i < 2; i++) ux[i] = ld16(rs, ub + (unsigned)((24 + 2 * v + i) * 1024));
      pace(tref, DR);
      STAMP(blockIdx.x, s, 2);
      const unsigned o[4] = {pb + (unsigned)((12 + v) * 1024 + lane * 16), pb + 16384u + (unsigned)(v * 1024 + lane * 16),
                             sb + ST_UM + (unsigned)(((c0 * 32 + 8 * v + g) * 16 + c16) * 16), sb + ST_UM + (unsigned)(((c0 * 32 + 8 * v + 4 + g) * 16 + c16) * 16)};
      poll16<4>(rs, o, pa, dead);
      acc[0] = smf(pa[0], ux[0], ux[1], acc[0], idx);
      acc[0] = smf(pa[1], pa[2], pa[3], acc[0], idx);
    }
    STAMP(blockIdx.x, s, 3);
#pragma unroll
    for (int t = 0; t < RC; t++) {
      float f0 = fold2(acc[t][0], acc[t][1]), f1 = fold2(acc[t][2], acc[t][3]);
      LAS float* red = (LAS float*)(lds + ((s & 1) * RC + t) * 4096) + w * 128 + (4 * (g & 1) + (g >> 1)) * 16 + c16;
      red[0] = f0; red[32] = f1;
    }
    wgbar();
#pragma unroll
    for (int t = 0; t < RC; t++) {
      const int r = lane >> 2, dq = lane & 3;
      const LAS f2* red = (const LAS f2*)((LAS float*)(lds + ((s & 1) * RC + t) * 4096) + (r & 7) * 16 + 4 * dq + 2 * (r >> 3));
      f2 a = red[0], b = red[64], c2 = red[128], d = red[192], e = red[256], f = red[320], gg = red[384], hh = red[448];
      f2 tt = ((a + b) + (c2 + d)) + ((e + f) + (gg + hh));
      st4(rs, sb + STEP_BYTES + ST_MEM + (unsigned)(w * 8192 + (c0 + t) * 256 + lane * 4), san(pk2(tt[0], tt[1])));
    }
    STAMP(blockIdx.x, s, 5);
    if (s == 0) STAMPR(2);
  }
  PROF_END; STAMPR(3);
}
#define SSL 127

constexpr int S_UC = 4288;
constexpr int S_RV = 4640;
constexpr int S_CH = 4 * S_UC + 2 * S_RV;
constexpr int S_KD = 4 * S_CH;
constexpr int LAGMAX = 2064;

__device__ __forceinline__ void cexp2(float a2, float brev, float L, float& re, float& im) {
  float mag = __builtin_amdgcn_exp2f(a2 * L);
  float ph = __builtin_amdgcn_fractf(brev * L);
  re = mag * __builtin_amdgcn_cosf(ph); im = mag * __builtin_amdgcn_sinf(ph);
}

__device__ __forceinline__ void ssm_unit(const Params& p, LAS unsigned char* lds, rsrc_t rs, int d0) {
  const int tid = threadIdx.x, lane = tid & 63, w = __builtin_amdgcn_readfirstlane(tid >> 6);
  const int c16 = lane & 15, g = lane >> 4, ch = w >> 1, hw = w & 1, d = d0 + ch;
  for (int i = tid; i < 4 * S_CH / 16; i += 512) *(LAS v4u*)(lds + i * 16) = v4u{0u, 0u, 0u, 0u};
  v2u xv[4];
#pragma unroll
  for (int i = 0; i < 4; i++) xv[i] = *(const v2u*)(p.x + (size_t)(i * 512 + tid) * 512 + d0);
  wgbar();
#pragma unroll
  for (int i = 0; i < 4; i++) {
    const int l = i * 512 + tid;
#pragma unroll
    for (int c = 0; c < 4; c++) {
      unsigned short v = (unsigned short)((c & 2 ? xv[i].y : xv[i].x) >> (16 * (c & 1)));
#pragma unroll
      for (int r = 0; r < 4; r++) *(LAS unsigned short*)(lds + c * S_CH + r * S_UC + (l + 32 - r) * 2) = v;
    }
  }
  {
    const float dt = rbf(expf(bf2f(p.logdt[d])));
    float L1[3];
    L1[0] = (float)((c16 & 7) + 128 * (c16 >> 3));
    L1[1] = (c16 < 8) ? (float)(256 + 128 * (c16 >> 2) + 2 * (c16 & 3)) : (float)(512 + 128 * ((c16 - 8) >> 1) + 4 * (c16 & 1));
    L1[2] = (float)(1024 + 128 * (c16 < 9 ? c16 : 0));
    const float L2 = (float)(8 * c16);
    f4 acc[3] = {f4{0.f, 0.f, 0.f, 0.f}, f4{0.f, 0.f, 0.f, 0.f}, f4{0.f, 0.f, 0.f, 0.f}};
    const int n0 = d * 128 + hw * 64 + g * 16;
    v4u pr[6][2];
#pragma unroll
    for (int q = 0; q < 2; q++) {
      pr[0][q] = *(const v4u*)(p.Are + n0 + 8 * q); pr[1][q] = *(const v4u*)(p.Aim + n0 + 8 * q);
      pr[2][q] = *(const v4u*)(p.Bre + n0 + 8 * q); pr[3][q] = *(const v4u*)(p.Bim + n0 + 8 * q);
      pr[4][q] = *(const v4u*)(p.Cre + n0 + 8 * q); pr[5][q] = *(const v4u*)(p.Cim + n0 + 8 * q);
    }
#pragma unroll
    for (int m = 0; m < 16; m++) {
#define PRM(k) __uint_as_float((m & 1) ? (pr[k][m >> 3][(m >> 1) & 3] & 0xFFFF0000u) : (pr[k][m >> 3][(m >> 1) & 3] << 16))
      const float a = dt * PRM(0), b = dt * PRM(1);
      const float br = dt * PRM(2), bi = dt * PRM(3), cr = PRM(4), ci = PRM(5);
#undef PRM
      const float cbr = cr * br - ci * bi, cbi = cr * bi + ci * br;
      const float a2 = a * 1.4426950408889634f, brev = b * 0.15915494309189535f;
      float yr, yi; cexp2(a2, brev, L2, yr, yi);
#pragma unroll
      for (int t = 0; t < 3; t++) {
        float er, ei; cexp2(a2, brev, L1[t], er, ei);
        float xr = cbr * er - cbi * ei, xi = cbr * ei + cbi * er;
        acc[t] = __builtin_amdgcn_mfma_f32_16x16x4f32(xr, yr, acc[t], 0, 0, 0);
        acc[t] = __builtin_amdgcn_mfma_f32_16x16x4f32(-xi, yi, acc[t], 0, 0, 0);
      }
    }
    LAS float* kd = (LAS float*)(lds + S_KD + w * 3072);
#pragma unroll
    for (int t = 0; t < 3; t++)
#pragma unroll
      for (int i = 0; i < 4; i++) kd[(16 * t + 4 * g + i) * 16 + c16] = acc[t][i];
  }
  wgbar();
#pragma unroll
  for (int i = 0; i < 4; i++) {
    const int l = i * 512 + tid;
    const int lr = (int)rbf((float)l);
    int row, j;
    if (lr < 256) { j = (lr >> 3) & 15; row = (lr & 7) + 8 * (lr >> 7); }
    else if (lr < 512) { int x = lr - 256; j = (x & 127) >> 3; row = 16 + 4 * (x >> 7) + ((x & 7) >> 1); }
    else if (lr < 1024) { int x = lr - 512; j = (x & 127) >> 3; row = 24 + 2 * (x >> 7) + ((x & 7) >> 2); }
    else { int x = lr - 1024; j = (x & 127) >> 3; row = 32 + (x >> 7); }
#pragma unroll
    for (int c = 0; c < 4; c++) {
      const LAS float* kd = (const LAS float*)(lds + S_KD + c * 6144);
      float k = kd[row * 16 + j] + kd[768 + row * 16 + j];
      float hi = rbf(k);
      LAS unsigned char* rv = lds + c * S_CH + 4 * S_UC + (LAGMAX - l) * 2;
      *(LAS unsigned short*)(rv) = f2bfu(hi);
      *(LAS unsigned short*)(rv + S_RV) = f2bfu(k - hi);
    }
  }
  wgbar();
  f4 acc[4];
#pragma unroll
  for (int t = 0; t < 4; t++) acc[t] = f4{0.f, 0.f, 0.f, 0.f};
  const int A0 = hw, A1 = 3 - hw, A2 = 4 + hw, A3 = 7 - hw;
  const LAS unsigned char* ap = lds + ch * S_CH + 4 * S_UC + (LAGMAX - 16 * c16 + 224 + 8 * g) * 2;
  const LAS unsigned char* bp = lds + ch * S_CH + (c16 & 3) * S_UC + ((c16 & 12) + 224 + 8 * g + 32) * 2;
#define SSM_STEP(T, A) { const LAS v2u* q = (const LAS v2u*)(bp + 512 * (A) - 64 * n); v2u b0 = q[0], b1 = q[1]; v4u bf = {b0.x, b0.y, b1.x, b1.y}; \
    acc[T] = mfma16(ah, bf, acc[T]); acc[T] = mfma16(al, bf, acc[T]); }
  int n = 0;
  for (; n <= 8 * A0 + 8; n++) { v4u ah = lds16(ap - 64 * n), al = lds16(ap - 64 * n + S_RV); SSM_STEP(0, A0) SSM_STEP(1, A1) SSM_STEP(2, A2) SSM_STEP(3, A3) }
  for (; n <= 8 * A1 + 8; n++) { v4u ah = lds16(ap - 64 * n), al = lds16(ap - 64 * n + S_RV); SSM_STEP(1, A1) SSM_STEP(2, A2) SSM_STEP(3, A3) }
  for (; n <= 8 * A2 + 8; n++) { v4u ah = lds16(ap - 64 * n), al = lds16(ap - 64 * n + S_RV); SSM_STEP(2, A2) SSM_STEP(3, A3) }
  for (; n <= 8 * A3 + 8; n++) { v4u ah = lds16(ap - 64 * n), al = lds16(ap - 64 * n + S_RV); SSM_STEP(3, A3) }
#undef SSM_STEP
  {
    const float dsk = bf2f(p.Dsk[d]);
#pragma unroll
    for (int t = 0; t < 4; t++) {
      const int A = t == 0 ? A0 : t == 1 ? A1 : t == 2 ? A2 : A3;
#pragma unroll
      for (int i = 0; i < 4; i++) {
        const int l = 256 * A + 16 * (4 * g + i) + c16;
        float u = bf2f(*(const LAS unsigned short*)(lds + ch * S_CH + (l + 32) * 2));
        float b = rbf(acc[t][i]) + rbf(u * dsk);
        *(LAS unsigned short*)(lds + S_KD + l * 8 + ch * 2) = f2bfu(b);
      }
    }
  }
  wgbar();
#pragma unroll
  for (int i = 0; i < 4; i++) {
    const int l = i * 512 + tid;
    st8(rs, OFF_YB + (unsigned)(l * 1024 + d0 * 2), *(const LAS v2u*)(lds + S_KD + l * 8));
  }
}

__device__ __forceinline__ void gate_unit(const Params& p, rsrc_t rs, int r0) {
  const int tid = threadIdx.x, lane = tid & 63, w = tid >> 6;
  v4u gw = *(const v4u*)(p.gw + lane * 8);
  for (int i = 0; i < 8; i++) {
    const int row = r0 + w * 8 + i;
    v4u xv = *(const v4u*)(p.x + (size_t)row * 512 + lane * 8);
    float s = 0.f;
#pragma unroll
    for (int k = 0; k < 4; k++) {
      s += rbf(__uint_as_float(xv[k] << 16) * __uint_as_float(gw[k] << 16));
      s += rbf(__uint_as_float(xv[k] & 0xFFFF0000u) * __uint_as_float(gw[k] & 0xFFFF0000u));
    }
#pragma unroll
    for (int o = 32; o > 0; o >>= 1) s += __shfl_xor(s, o);
    s = rbf(s);
    float gg = rbf(1.f / (1.f + expf(-s)));
    if (lane == 0) st4(rs, OFF_G + (unsigned)row * 4u, __float_as_uint(gg));
  }
}

__device__ __forceinline__ void out_unit(const Params& p, LAS unsigned char* lds, rsrc_t rs, int s, int cg, bool& dead) {
  const int tid = threadIdx.x, lane = tid & 63, w = __builtin_amdgcn_readfirstlane(tid >> 6);
  const int c16 = lane & 15, g = lane >> 4;
  const unsigned sb = OFF_STEP + (unsigned)s * STEP_BYTES;
#define OSL 254
  if (w == 0) {
    if (s < 57) wait_flags<OSL>(rs, OFF_UXF + (unsigned)(s * 32 + (lane & 31)) * FS, dead);
    else { wait_flags<OSL>(rs, OFF_UXF + (unsigned)(55 * 32 + (lane & 31)) * FS, dead); wait_flags<32>(rs, OFF_UXF + (unsigned)(s * 32 + (lane & 31)) * FS, dead); }
  }
  wgbar();
  v4u ux[4][4];
#pragma unroll
  for (int ct = 0; ct < 4; ct++)
#pragma unroll
    for (int i = 0; i < 4; i++) ux[ct][i] = ld16(rs, OFF_UX + (unsigned)s * 1048576u + (unsigned)(((4 * cg + ct) * 32 + 4 * w + i) * 1024 + lane * 16));
  const int erow = tid >> 4, ecol = 64 * cg + 4 * (tid & 15);
  const v2u yb = ld8(rs, OFF_YB + (unsigned)((32 * s + erow) * 1024 + ecol * 2));
  const float gg = __uint_as_float(ld4(rs, OFF_G + (unsigned)(32 * s + erow) * 4u));
  if (w == 0) {
    if (s < 57) wait_flags<OSL>(rs, OFF_PXF + (unsigned)(s * 64 + lane) * FS, dead);
    else if (s < 62) wait_flags<32>(rs, OFF_PXF + (unsigned)(s * 64 + lane) * FS, dead);
    else wait_flags<4>(rs, OFF_PXF + (unsigned)(62 * 64 + lane) * FS, dead);
  }
  wgbar();
  const unsigned xb = OFF_PX + (unsigned)s * 81920u;
  v4u pa[14];
  {
    unsigned o[14];
#pragma unroll
    for (int rt = 0; rt < 2; rt++) {
#pragma unroll
      for (int i = 0; i < 4; i++) o[rt * 5 + i] = xb + (unsigned)(((4 * w + i) * 2 + rt) * 1024 + lane * 16);
      o[rt * 5 + 4] = xb + 65536u + (unsigned)((w * 2 + rt) * 1024 + lane * 16);
    }
#pragma unroll
    for (int ct = 0; ct < 4; ct++) o[10 + ct] = sb + ST_UM + (unsigned)((((4 * cg + ct) * 32 + 4 * w + g) * 16 + c16) * 16);
    poll16<14>(rs, o, pa, dead);
  }
#pragma unroll
  for (int rt = 0; rt < 2; rt++)
#pragma unroll
    for (int ct = 0; ct < 4; ct++) {
      f4 acc = {0.f, 0.f, 0.f, 0.f};
#pragma unroll
      for (int i = 0; i < 4; i++) acc = mfma16(pa[rt * 5 + i], ux[ct][i], acc);
      acc = mfma16(pa[rt * 5 + 4], pa[10 + ct], acc);
      *(LAS f4*)(lds + w * 8192 + (rt * 4 + ct) * 1024 + lane * 16) = acc;
    }
  wgbar();
  {
    f4 t = *(const LAS f4*)(lds + tid * 16);
#pragma unroll
    for (int k = 1; k < 8; k++) t += *(const LAS f4*)(lds + k * 8192 + tid * 16);
    const int rt = w >> 2, ct = w & 3;
    LAS float* at = (LAS float*)(lds + 65536);
#pragma unroll
    for (int i = 0; i < 4; i++) at[(16 * rt + 4 * g + i) * 68 + 16 * ct + c16] = t[i];
  }
  wgbar();
  {
    f4 a = *(const LAS f4*)(lds + 65536 + (erow * 68 + 4 * (tid & 15)) * 4);
    const float og = rbf(1.f - gg);
    float b0 = __uint_as_float(yb.x << 16), b1 = __uint_as_float(yb.x & 0xFFFF0000u);
    float b2 = __uint_as_float(yb.y << 16), b3 = __uint_as_float(yb.y & 0xFFFF0000u);
    float o0 = rbf(gg * rbf(a[0])) + rbf(og * b0), o1 = rbf(gg * rbf(a[1])) + rbf(og * b1);
    float o2 = rbf(gg * rbf(a[2])) + rbf(og * b2), o3 = rbf(gg * rbf(a[3])) + rbf(og * b3);
    *(v2u*)(p.out + (size_t)(32 * s + erow) * 512 + ecol) = v2u{pk2(o0, o1), pk2(o2, o3)};
  }
}

__device__ __forceinline__ void role_spare(const Params& p, LAS unsigned char* lds, rsrc_t rs, int j) {
  const int tid = threadIdx.x, w = __builtin_amdgcn_readfirstlane(tid >> 6);
  bool dead = false;
  const v4u sv = {SENT, SENT, SENT, SENT};
  STAMPR(0);
  constexpr unsigned N0 = INIT0_STEPS * STEP_BYTES / 16, N1 = 64u * STEP_BYTES / 16;
  const unsigned tref = now32();
  for (unsigned i = (unsigned)(j * 512 + tid); i < N0; i += NSPARE * 512) st16(rs, OFF_STEP + i * 16u, sv);
  if (WARM) warm_weights(p, j & 7, 4 + (j >> 3));
  drain(); wgbar();
  if (tid == 0) st4(rs, OFF_CTR + CTR_INIT0 + (unsigned)j * 4u, 1u);
  STAMPR(1);
#define SSMFIRST 0
  if (DLY1) { pace(tref, DLY1 / 2); pace(tref, DLY1); }
  if (SSMFIRST) {
    if (j < 128) ssm_unit(p, lds, rs, 4 * j); else gate_unit(p, rs, 64 * (j - 128));
  }
  for (unsigned i = N0 + (unsigned)(j * 512 + tid); i < N1; i += NSPARE * 512) st16(rs, OFF_STEP + i * 16u, sv);
  if (j < 10) st16(rs, OFF_PX + 63u * 81920u + (unsigned)(j * 512 + tid) * 16u, sv);
  drain(); wgbar();
  if (tid == 0) st4(rs, OFF_CTR + CTR_INIT1 + (unsigned)j * 4u, 1u);
  STAMPR(2);
  if (!SSMFIRST) {
    if (j < 128) ssm_unit(p, lds, rs, 4 * j); else gate_unit(p, rs, 64 * (j - 128));
    drain(); wgbar();
  }
  if (tid == 0) st4(rs, OFF_CTR + CTR_SSM + (unsigned)j * 4u, 1u);
  STAMPR(3);
  if (w == 0) { wait_ctr<SSL>(rs, OFF_CTR + CTR_SSM, dead); wait_ctr<SSL>(rs, OFF_CTR + CTR_INIT1, dead); }
  wgbar();
  STAMPR(4);
  for (int u = j; u < 512; u += NSPARE) { out_unit(p, lds, rs, u >> 3, u & 7, dead); STAMPR(5); }
}

__global__ void __launch_bounds__(512, 2) fused_kernel(Params p) {
  extern __shared__ __attribute__((aligned(16))) unsigned char smem[];
  LAS unsigned char* lds = (LAS unsigned char*)smem;
  const rsrc_t rs = __builtin_amdgcn_make_buffer_rsrc((void*)p.ws, 0, (int)WS_BYTES, 0x00020000);
  const int b = blockIdx.x;
  if (b < 32) role_qk(p, lds, rs, b);
  else if (b < 64) role_vo(p, lds, rs, b - 32);
  else if (b < 96) role_r(p, lds, rs, b - 64);
  else role_spare(p, lds, rs, b - 96);
}

extern "C" void kernel_launch(void* const* d_in, const int* in_sizes, int n_in, void* d_out, int out_size,
                              void* d_ws, size_t ws_size, hipStream_t stream) {
  Params p;
  p.x = (const unsigned short*)d_in[0]; p.mem0 = (const unsigned short*)d_in[1];
  p.Wq = (const unsigned short*)d_in[2]; p.Wk = (const unsigned short*)d_in[3];
  p.Wv = (const unsigned short*)d_in[4]; p.Wo = (const unsigned short*)d_in[5];
  p.Are = (const unsigned short*)d_in[8]; p.Aim = (const unsigned short*)d_in[9];
  p.Bre = (const unsigned short*)d_in[10]; p.Bim = (const unsigned short*)d_in[11];
  p.Cre = (const unsigned short*)d_in[12]; p.Cim = (const unsigned short*)d_in[13];
  p.logdt = (const unsigned short*)d_in[14]; p.Dsk = (const unsigned short*)d_in[15];
  p.gw = (const unsigned short*)d_in[16];
  p.out = (unsigned short*)d_out; p.ws = (unsigned char*)d_ws;
  (void)hipFuncSetAttribute((const void*)fused_kernel, hipFuncAttributeMaxDynamicSharedMemorySize, LDS_BYTES);
  (void)hipMemsetAsync(d_ws, 0, CTR_BYTES, stream);
  fused_kernel<<<256, 512, LDS_BYTES, stream>>>(p);
}
```
